```python
import torch

def _softmax(x, axis=-1):
    return torch.softmax(x, dim=axis)
def _gelu(x):
    return torch.nn.functional.gelu(x, approximate="tanh")
def _silu(x):
    return torch.nn.functional.silu(x)
def _sigmoid(x):
    return torch.sigmoid(x)
def _layernorm(x, axis=-1, eps=1e-5):
    m = x.mean(dim=axis, keepdim=True)
    v = x.var(dim=axis, keepdim=True, unbiased=False)
    return (x - m) / torch.sqrt(v + eps)
def _scatter_add(tgt, idx, src, axis=0):
    return tgt.clone().index_add_(axis, idx, src)
def _segment_sum(src, seg, n):
    out = torch.zeros((n,) + src.shape[1:], dtype=src.dtype)
    return out.index_add_(0, seg, src)
def _pad(x, pw):
    flat = tuple(v for pair in reversed(pw) for v in pair)
    return torch.nn.functional.pad(x, flat)

kernel_name = 'standalone__weight_only_int_quip__prompt'

def setup_inputs():

    _w = (lambda a: (lambda t: t.to(torch.bfloat16) if t.is_floating_point() else t)(torch.as_tensor(a)))
    B, S, D, F = 1, 2048, 1024, 3584
    G = 32 if 32 > 0 else D
    D = (D // G) * G
    ng = D // G
    bits = 8 if 8 > 0 else 4
    qmax = 1 << bits
    qw = torch.randint(0, qmax, (D, F)).to(torch.int32)
    U, _ = (lambda *_a, **_k: torch.linalg.qr(*[torch.as_tensor(_x).float() for _x in _a], **_k))((lambda *s: torch.randn(*s, dtype=torch.bfloat16) * 0.05)(D, D))
    V, _ = (lambda *_a, **_k: torch.linalg.qr(*[torch.as_tensor(_x).float() for _x in _a], **_k))((lambda *s: torch.randn(*s, dtype=torch.bfloat16) * 0.05)(F, F))
    return {
        "x": _w((lambda *s: torch.randn(*s, dtype=torch.bfloat16) * 0.05)(B, S, D)),
        "qw": _w(qw),
        "scales": _w(torch.abs((lambda *s: torch.randn(*s, dtype=torch.bfloat16) * 0.05)(ng, F)) * 0.02 + 0.005),
        "zeros": _w(torch.full((ng, F), qmax / 2.0, dtype=float)),
        "U": _w(U.to(float)),
        "V": _w(V.to(float)),
    }

def reference(x, qw, scales, zeros, U, V):
    B, S, D = x.shape
    ng, F = scales.shape
    G = D // ng
    s = (lambda x, n, axis: torch.repeat_interleave(x, n, dim=axis))(scales, G, axis=0)
    z = (lambda x, n, axis: torch.repeat_interleave(x, n, dim=axis))(zeros, G, axis=0)
    wq = ((lambda x, dt: x.to(dt))(qw, x.dtype) - z) * s
    xr = torch.einsum("bsd,de->bse", x, U)
    h  = torch.einsum("bse,ef->bsf", xr, wq)
    y  = torch.einsum("bsf,fg->bsg", h, V)
    return y
```

```cpp
#include <hip/hip_runtime.h>
#include <hip/hip_bf16.h>
typedef unsigned short u16;
using bf16x8 = __attribute__((ext_vector_type(8))) short;
using f32x4  = __attribute__((ext_vector_type(4))) float;
__device__ inline float bf2f(u16 b){ return __uint_as_float(((unsigned)b)<<16); }
__device__ inline u16 f2bf(float f){ __hip_bfloat16 h = __float2bfloat16(f); return *(u16*)&h; }

__global__ void dequant(const int* qw, const u16* sc, const u16* zr, u16* wq, int D, int F){
  int i = blockIdx.x*blockDim.x+threadIdx.x; if(i>=D*F) return;
  int d=i/F, f=i%F; int g=d/32;
  float q = bf2f(f2bf((float)qw[i]));
  float t = bf2f(f2bf(q - bf2f(zr[g*F+f])));
  wq[i] = f2bf(t*bf2f(sc[g*F+f]));
}
__global__ __launch_bounds__(64) void gemm(const u16* A, const u16* B, u16* C, int M, int N, int K){
  __shared__ u16 sA[16][32]; __shared__ u16 sB[32][16];
  int bx=blockIdx.x*16, by=blockIdx.y*16, lane=threadIdx.x, row=lane&15, quad=lane>>4;
  f32x4 acc={0,0,0,0};
  for(int k0=0;k0<K;k0+=32){
    for(int e=lane;e<512;e+=64){ int r=e/32,c=e%32; sA[r][c]=A[(long)(by+r)*K+k0+c]; int r2=e%16,c2=e/16; sB[c2][r2]=B[(long)(k0+c2)*N+bx+r2]; }
    __syncthreads();
    bf16x8 a,b;
    for(int j=0;j<8;++j){ a[j]=(short)sA[row][quad*8+j]; b[j]=(short)sB[quad*8+j][row]; }
    acc=__builtin_amdgcn_mfma_f32_16x16x32_bf16(a,b,acc,0,0,0);
    __syncthreads();
  }
  for(int j=0;j<4;++j) C[(long)(by+quad*4+j)*N+bx+row]=f2bf(acc[j]);
}
extern "C" void kernel_launch(void* const* d_in, const int* in_sizes, int n_in, void* d_out, int out_size, void* d_ws, size_t ws_size, hipStream_t stream){
  const int S=2048,D=1024,F=3584;
  const u16* x=(const u16*)d_in[0]; const int* qw=(const int*)d_in[1]; const u16* sc=(const u16*)d_in[2]; const u16* zr=(const u16*)d_in[3];
  const u16* U=(const u16*)d_in[4]; const u16* V=(const u16*)d_in[5];
  u16* wq=(u16*)d_ws; u16* xr=wq+(size_t)D*F; u16* wv=xr+(size_t)S*D;
  dequant<<<(D*F+255)/256,256,0,stream>>>(qw,sc,zr,wq,D,F);
  gemm<<<dim3(D/16,S/16),64,0,stream>>>(x,U,xr,S,D,D);
  gemm<<<dim3(F/16,D/16),64,0,stream>>>(wq,V,wv,D,F,F);
  gemm<<<dim3(F/16,S/16),64,0,stream>>>(xr,wv,(u16*)d_out,S,F,D);
}
```

```cpp
#define FS0 8
#define ENT 1
#include <hip/hip_runtime.h>
typedef unsigned short u16;
typedef unsigned long long u64;
typedef float f32x16 __attribute__((ext_vector_type(16)));
typedef float f32x2 __attribute__((ext_vector_type(2)));
typedef unsigned u32x4 __attribute__((ext_vector_type(4)));
typedef unsigned u32x2 __attribute__((ext_vector_type(2)));
typedef int i32x8 __attribute__((ext_vector_type(8)));
typedef unsigned u32x8 __attribute__((ext_vector_type(8)));
typedef int i32x4 __attribute__((ext_vector_type(4)));
typedef __bf16 bf2_t __attribute__((ext_vector_type(2)));
#define LAS __attribute__((address_space(3)))
#define GAS __attribute__((address_space(1)))
#define DI __device__ __forceinline__
#define WAITV(n) asm volatile("s_waitcnt vmcnt(%0)"::"i"(n):"memory")
#define BAR do{ asm volatile("s_barrier":::"memory"); }while(0)

constexpr int WST=24, CTAIL=1, AQ=0, EV=0;
#define ASM_A \
  "s_waitcnt lgkmcnt(0)\n" \
  "ds_read_b64_tr_b16 v[32:33], %[b0] offset:0\n" \
  "ds_read_b64_tr_b16 v[34:35], %[b0] offset:1024\n" \
  "ds_read_b128 v[48:51], %[a0] offset:0\n" \
  "ds_read_b64_tr_b16 v[36:37], %[b1] offset:0\n" \
  "ds_read_b64_tr_b16 v[38:39], %[b1] offset:1024\n" \
  "ds_read_b64_tr_b16 v[40:41], %[b2] offset:0\n" \
  "ds_read_b64_tr_b16 v[42:43], %[b2] offset:1024\n" \
  "ds_read_b64_tr_b16 v[44:45], %[b3] offset:0\n" \
  "ds_read_b64_tr_b16 v[46:47], %[b3] offset:1024\n" \
  "ds_read_b128 v[52:55], %[a0] offset:2048\n" \
  "ds_read_b128 v[56:59], %[a0] offset:4096\n" \
  "ds_read_b128 v[60:63], %[a0] offset:6144\n" \
  "ds_read_b128 v[64:67], %[a0] offset:16384\n" \
  "ds_read_b128 v[68:71], %[a0] offset:18432\n" \
  "ds_read_b128 v[72:75], %[a0] offset:20480\n" \
  "ds_read_b128 v[76:79], %[a0] offset:22528\n" \
  "s_waitcnt lgkmcnt(13)\n" \
  "v_mfma_f32_16x16x32_bf16 v[128:131], v[32:35], v[48:51], 0\n" \
  "ds_read_b64_tr_b16 v[80:81], %[b0] offset:8192\n" \
  "s_waitcnt lgkmcnt(12)\n" \
  "v_mfma_f32_16x16x32_bf16 v[132:135], v[36:39], v[48:51], 0\n" \
  "ds_read_b64_tr_b16 v[82:83], %[b0] offset:9216\n" \
  "s_waitcnt lgkmcnt(11)\n" \
  "v_mfma_f32_16x16x32_bf16 v[136:139], v[40:43], v[48:51], 0\n" \
  "ds_read_b128 v[96:99], %[a1] offset:0\n" \
  "s_add_u32 %[slot], %[slot], 1\n" \
  "s_cmp_eq_u32 %[slot], 3\n" \
  "s_waitcnt lgkmcnt(10)\n" \
  "v_mfma_f32_16x16x32_bf16 v[140:143], v[44:47], v[48:51], 0\n" \
  "ds_read_b64_tr_b16 v[84:85], %[b1] offset:8192\n" \
  "s_cselect_b32 %[dA], %[kW], %[kS]\n" \
  "s_cselect_b32 %[dB], %[kWB], %[kSB]\n" \
  "s_cselect_b32 %[slot], 0, %[slot]\n" \
  "s_waitcnt lgkmcnt(10)\n" \
  "v_mfma_f32_16x16x32_bf16 v[156:159], v[44:47], v[52:55], 0\n" \
  "ds_read_b64_tr_b16 v[86:87], %[b1] offset:9216\n" \
  "v_mfma_f32_16x16x32_bf16 v[152:155], v[40:43], v[52:55], 0\n" \
  "ds_read_b64_tr_b16 v[88:89], %[b2] offset:8192\n" \
  "v_mfma_f32_16x16x32_bf16 v[148:151], v[36:39], v[52:55], 0\n" \
  "ds_read_b64_tr_b16 v[90:91], %[b2] offset:9216\n" \
  "v_mfma_f32_16x16x32_bf16 v[144:147], v[32:35], v[52:55], 0\n" \
  "ds_read_b64_tr_b16 v[92:93], %[b3] offset:8192\n" \
  "s_waitcnt lgkmcnt(13)\n" \
  "v_mfma_f32_16x16x32_bf16 v[160:163], v[32:35], v[56:59], 0\n" \
  "ds_read_b64_tr_b16 v[94:95], %[b3] offset:9216\n" \
  "v_mfma_f32_16x16x32_bf16 v[164:167], v[36:39], v[56:59], 0\n" \
  "ds_read_b128 v[100:103], %[a1] offset:2048\n" \
  "v_mfma_f32_16x16x32_bf16 v[168:171], v[40:43], v[56:59], 0\n" \
  "ds_read_b128 v[104:107], %[a1] offset:4096\n" \
  "v_mfma_f32_16x16x32_bf16 v[172:175], v[44:47], v[56:59], 0\n" \
  "ds_read_b128 v[108:111], %[a1] offset:6144\n" \
  "s_waitcnt lgkmcnt(15)\n" \
  "v_mfma_f32_16x16x32_bf16 v[188:191], v[44:47], v[60:63], 0\n" \
  "ds_read_b128 v[112:115], %[a1] offset:16384\n" \
  "v_mfma_f32_16x16x32_bf16 v[184:187], v[40:43], v[60:63], 0\n" \
  "ds_read_b128 v[116:119], %[a1] offset:18432\n" \
  "v_mfma_f32_16x16x32_bf16 v[180:183], v[36:39], v[60:63], 0\n" \
  "ds_read_b128 v[120:123], %[a1] offset:20480\n" \
  "v_mfma_f32_16x16x32_bf16 v[176:179], v[32:35], v[60:63], 0\n" \
  "ds_read_b128 v[124:127], %[a1] offset:22528\n" \
  "v_mfma_f32_16x16x32_bf16 v[192:195], v[32:35], v[64:67], 0\n" \
  "v_add_u32 %[b0], %[dB], %[b0]\n" \
  "v_mfma_f32_16x16x32_bf16 v[196:199], v[36:39], v[64:67], 0\n" \
  "v_add_u32 %[b1], %[dB], %[b1]\n" \
  "v_mfma_f32_16x16x32_bf16 v[200:203], v[40:43], v[64:67], 0\n" \
  "v_add_u32 %[b2], %[dB], %[b2]\n" \
  "v_mfma_f32_16x16x32_bf16 v[204:207], v[44:47], v[64:67], 0\n" \
  "v_add_u32 %[b3], %[dB], %[b3]\n" \
  "s_waitcnt lgkmcnt(15)\n" \
  "v_mfma_f32_16x16x32_bf16 v[220:223], v[44:47], v[68:71], 0\n" \
  "v_add_u32 %[a0], %[dA], %[a0]\n" \
  "v_mfma_f32_16x16x32_bf16 v[216:219], v[40:43], v[68:71], 0\n" \
  "v_add_u32 %[a1], %[dA], %[a1]\n" \
  "v_mfma_f32_16x16x32_bf16 v[212:215], v[36:39], v[68:71], 0\n" \
  "v_mfma_f32_16x16x32_bf16 v[208:211], v[32:35], v[68:71], 0\n" \
  "v_mfma_f32_16x16x32_bf16 v[224:227], v[32:35], v[72:75], 0\n" \
  "v_mfma_f32_16x16x32_bf16 v[228:231], v[36:39], v[72:75], 0\n" \
  "v_mfma_f32_16x16x32_bf16 v[232:235], v[40:43], v[72:75], 0\n" \
  "v_mfma_f32_16x16x32_bf16 v[236:239], v[44:47], v[72:75], 0\n" \
  "v_mfma_f32_16x16x32_bf16 v[252:255], v[44:47], v[76:79], 0\n" \
  "v_mfma_f32_16x16x32_bf16 v[248:251], v[40:43], v[76:79], 0\n" \
  "v_mfma_f32_16x16x32_bf16 v[244:247], v[36:39], v[76:79], 0\n" \
  "v_mfma_f32_16x16x32_bf16 v[240:243], v[32:35], v[76:79], 0\n" \
  "s_branch 2f\n" \
  ".p2align 6\n" \
  "s_nop 0\n" \
  "s_nop 0\n" \
  "s_nop 0\n" \
  "s_nop 0\n" \
  "s_nop 0\n" \
  "s_nop 0\n" \
  "s_nop 0\n" \
  "s_nop 0\n" \
  "s_nop 0\n" \
  "s_nop 0\n" \
  "1:\n" \
  "s_waitcnt lgkmcnt(13)\n" \
  "v_mfma_f32_16x16x32_bf16 v[128:131], v[32:35], v[48:51], v[128:131]\n" \
  "ds_read_b64_tr_b16 v[80:81], %[b0] offset:8192\n" \
  "s_waitcnt lgkmcnt(12)\n" \
  "v_mfma_f32_16x16x32_bf16 v[132:135], v[36:39], v[48:51], v[132:135]\n" \
  "ds_read_b64_tr_b16 v[82:83], %[b0] offset:9216\n" \
  "s_waitcnt lgkmcnt(11)\n" \
  "v_mfma_f32_16x16x32_bf16 v[136:139], v[40:43], v[48:51], v[136:139]\n" \
  "ds_read_b128 v[96:99], %[a1] offset:0\n" \
  "s_add_u32 %[slot], %[slot], 1\n" \
  "s_cmp_eq_u32 %[slot], 3\n" \
  "s_waitcnt lgkmcnt(10)\n" \
  "v_mfma_f32_16x16x32_bf16 v[140:143], v[44:47], v[48:51], v[140:143]\n" \
  "ds_read_b64_tr_b16 v[84:85], %[b1] offset:8192\n" \
  "s_cselect_b32 %[dA], %[kW], %[kS]\n" \
  "s_cselect_b32 %[dB], %[kWB], %[kSB]\n" \
  "s_cselect_b32 %[slot], 0, %[slot]\n" \
  "s_waitcnt lgkmcnt(10)\n" \
  "v_mfma_f32_16x16x32_bf16 v[156:159], v[44:47], v[52:55], v[156:159]\n" \
  "ds_read_b64_tr_b16 v[86:87], %[b1] offset:9216\n" \
  "v_mfma_f32_16x16x32_bf16 v[152:155], v[40:43], v[52:55], v[152:155]\n" \
  "ds_read_b64_tr_b16 v[88:89], %[b2] offset:8192\n" \
  "v_mfma_f32_16x16x32_bf16 v[148:151], v[36:39], v[52:55], v[148:151]\n" \
  "ds_read_b64_tr_b16 v[90:91], %[b2] offset:9216\n" \
  "v_mfma_f32_16x16x32_bf16 v[144:147], v[32:35], v[52:55], v[144:147]\n" \
  "ds_read_b64_tr_b16 v[92:93], %[b3] offset:8192\n" \
  "s_waitcnt lgkmcnt(13)\n" \
  "v_mfma_f32_16x16x32_bf16 v[160:163], v[32:35], v[56:59], v[160:163]\n" \
  "ds_read_b64_tr_b16 v[94:95], %[b3] offset:9216\n" \
  "v_mfma_f32_16x16x32_bf16 v[164:167], v[36:39], v[56:59], v[164:167]\n" \
  "ds_read_b128 v[100:103], %[a1] offset:2048\n" \
  "v_mfma_f32_16x16x32_bf16 v[168:171], v[40:43], v[56:59], v[168:171]\n" \
  "ds_read_b128 v[104:107], %[a1] offset:4096\n" \
  "v_mfma_f32_16x16x32_bf16 v[172:175], v[44:47], v[56:59], v[172:175]\n" \
  "ds_read_b128 v[108:111], %[a1] offset:6144\n" \
  "s_waitcnt lgkmcnt(15)\n" \
  "v_mfma_f32_16x16x32_bf16 v[188:191], v[44:47], v[60:63], v[188:191]\n" \
  "ds_read_b128 v[112:115], %[a1] offset:16384\n" \
  "v_mfma_f32_16x16x32_bf16 v[184:187], v[40:43], v[60:63], v[184:187]\n" \
  "ds_read_b128 v[116:119], %[a1] offset:18432\n" \
  "v_mfma_f32_16x16x32_bf16 v[180:183], v[36:39], v[60:63], v[180:183]\n" \
  "ds_read_b128 v[120:123], %[a1] offset:20480\n" \
  "v_mfma_f32_16x16x32_bf16 v[176:179], v[32:35], v[60:63], v[176:179]\n" \
  "ds_read_b128 v[124:127], %[a1] offset:22528\n" \
  "v_mfma_f32_16x16x32_bf16 v[192:195], v[32:35], v[64:67], v[192:195]\n" \
  "v_add_u32 %[b0], %[dB], %[b0]\n" \
  "v_mfma_f32_16x16x32_bf16 v[196:199], v[36:39], v[64:67], v[196:199]\n" \
  "v_add_u32 %[b1], %[dB], %[b1]\n" \
  "v_mfma_f32_16x16x32_bf16 v[200:203], v[40:43], v[64:67], v[200:203]\n" \
  "v_add_u32 %[b2], %[dB], %[b2]\n" \
  "v_mfma_f32_16x16x32_bf16 v[204:207], v[44:47], v[64:67], v[204:207]\n" \
  "v_add_u32 %[b3], %[dB], %[b3]\n" \
  "s_waitcnt lgkmcnt(15)\n" \
  "v_mfma_f32_16x16x32_bf16 v[220:223], v[44:47], v[68:71], v[220:223]\n" \
  "v_add_u32 %[a0], %[dA], %[a0]\n" \
  "v_mfma_f32_16x16x32_bf16 v[216:219], v[40:43], v[68:71], v[216:219]\n" \
  "v_add_u32 %[a1], %[dA], %[a1]\n" \
  "v_mfma_f32_16x16x32_bf16 v[212:215], v[36:39], v[68:71], v[212:215]\n" \
  "v_mfma_f32_16x16x32_bf16 v[208:211], v[32:35], v[68:71], v[208:211]\n" \
  "v_mfma_f32_16x16x32_bf16 v[224:227], v[32:35], v[72:75], v[224:227]\n" \
  "v_mfma_f32_16x16x32_bf16 v[228:231], v[36:39], v[72:75], v[228:231]\n" \
  "v_mfma_f32_16x16x32_bf16 v[232:235], v[40:43], v[72:75], v[232:235]\n" \
  "v_mfma_f32_16x16x32_bf16 v[236:239], v[44:47], v[72:75], v[236:239]\n" \
  "v_mfma_f32_16x16x32_bf16 v[252:255], v[44:47], v[76:79], v[252:255]\n" \
  "v_mfma_f32_16x16x32_bf16 v[248:251], v[40:43], v[76:79], v[248:251]\n" \
  "v_mfma_f32_16x16x32_bf16 v[244:247], v[36:39], v[76:79], v[244:247]\n" \
  "v_mfma_f32_16x16x32_bf16 v[240:243], v[32:35], v[76:79], v[240:243]\n" \
  "2:\n" \
  "s_waitcnt lgkmcnt(13)\n" \
  "v_mfma_f32_16x16x32_bf16 v[128:131], v[80:83], v[96:99], v[128:131]\n" \
  "s_waitcnt lgkmcnt(11)\n" \
  "v_mfma_f32_16x16x32_bf16 v[132:135], v[84:87], v[96:99], v[132:135]\n" \
  "s_waitcnt lgkmcnt(9)\n" \
  "v_mfma_f32_16x16x32_bf16 v[136:139], v[88:91], v[96:99], v[136:139]\n" \
  "s_waitcnt lgkmcnt(7)\n" \
  "v_mfma_f32_16x16x32_bf16 v[140:143], v[92:95], v[96:99], v[140:143]\n" \
  "s_waitcnt lgkmcnt(6)\n" \
  "v_mfma_f32_16x16x32_bf16 v[156:159], v[92:95], v[100:103], v[156:159]\n" \
  "v_mfma_f32_16x16x32_bf16 v[152:155], v[88:91], v[100:103], v[152:155]\n" \
  "v_mfma_f32_16x16x32_bf16 v[148:151], v[84:87], v[100:103], v[148:151]\n" \
  "v_mfma_f32_16x16x32_bf16 v[144:147], v[80:83], v[100:103], v[144:147]\n" \
  "s_waitcnt lgkmcnt(0)\n" \
  "s_barrier\n" \
  "v_mfma_f32_16x16x32_bf16 v[160:163], v[80:83], v[104:107], v[160:163]\n" \
  "ds_read_b64_tr_b16 v[32:33], %[b0] offset:0\n" \
  "v_mfma_f32_16x16x32_bf16 v[164:167], v[84:87], v[104:107], v[164:167]\n" \
  "ds_read_b64_tr_b16 v[34:35], %[b0] offset:1024\n" \
  "v_mfma_f32_16x16x32_bf16 v[168:171], v[88:91], v[104:107], v[168:171]\n" \
  "ds_read_b128 v[48:51], %[a0] offset:0\n" \
  "v_mfma_f32_16x16x32_bf16 v[172:175], v[92:95], v[104:107], v[172:175]\n" \
  "ds_read_b64_tr_b16 v[36:37], %[b1] offset:0\n" \
  "v_mfma_f32_16x16x32_bf16 v[188:191], v[92:95], v[108:111], v[188:191]\n" \
  "ds_read_b64_tr_b16 v[38:39], %[b1] offset:1024\n" \
  "v_mfma_f32_16x16x32_bf16 v[184:187], v[88:91], v[108:111], v[184:187]\n" \
  "ds_read_b64_tr_b16 v[40:41], %[b2] offset:0\n" \
  "v_mfma_f32_16x16x32_bf16 v[180:183], v[84:87], v[108:111], v[180:183]\n" \
  "ds_read_b64_tr_b16 v[42:43], %[b2] offset:1024\n" \
  "v_mfma_f32_16x16x32_bf16 v[176:179], v[80:83], v[108:111], v[176:179]\n" \
  "ds_read_b64_tr_b16 v[44:45], %[b3] offset:0\n" \
  "v_mfma_f32_16x16x32_bf16 v[192:195], v[80:83], v[112:115], v[192:195]\n" \
  "ds_read_b64_tr_b16 v[46:47], %[b3] offset:1024\n" \
  "v_mfma_f32_16x16x32_bf16 v[196:199], v[84:87], v[112:115], v[196:199]\n" \
  "ds_read_b128 v[52:55], %[a0] offset:2048\n" \
  "v_mfma_f32_16x16x32_bf16 v[200:203], v[88:91], v[112:115], v[200:203]\n" \
  "ds_read_b128 v[56:59], %[a0] offset:4096\n" \
  "v_mfma_f32_16x16x32_bf16 v[204:207], v[92:95], v[112:115], v[204:207]\n" \
  "ds_read_b128 v[60:63], %[a0] offset:6144\n" \
  "v_mfma_f32_16x16x32_bf16 v[220:223], v[92:95], v[116:119], v[220:223]\n" \
  "ds_read_b128 v[64:67], %[a0] offset:16384\n" \
  "v_mfma_f32_16x16x32_bf16 v[216:219], v[88:91], v[116:119], v[216:219]\n" \
  "ds_read_b128 v[68:71], %[a0] offset:18432\n" \
  "v_mfma_f32_16x16x32_bf16 v[212:215], v[84:87], v[116:119], v[212:215]\n" \
  "ds_read_b128 v[72:75], %[a0] offset:20480\n" \
  "v_mfma_f32_16x16x32_bf16 v[208:211], v[80:83], v[116:119], v[208:211]\n" \
  "ds_read_b128 v[76:79], %[a0] offset:22528\n" \
  "v_mfma_f32_16x16x32_bf16 v[224:227], v[80:83], v[120:123], v[224:227]\n" \
  "v_mfma_f32_16x16x32_bf16 v[228:231], v[84:87], v[120:123], v[228:231]\n" \
  "s_cmp_eq_u32 %[cnt], %[sd]\n" \
  "s_cbranch_scc1 3f\n" \
  "4:\n" \
  "v_mfma_f32_16x16x32_bf16 v[232:235], v[88:91], v[120:123], v[232:235]\n" \
  "v_mfma_f32_16x16x32_bf16 v[236:239], v[92:95], v[120:123], v[236:239]\n" \
  "v_mfma_f32_16x16x32_bf16 v[252:255], v[92:95], v[124:127], v[252:255]\n" \
  "s_sub_u32 %[cnt], %[cnt], 1\n" \
  "s_cmp_lg_u32 %[cnt], 0\n" \
  "v_mfma_f32_16x16x32_bf16 v[248:251], v[88:91], v[124:127], v[248:251]\n" \
  "v_mfma_f32_16x16x32_bf16 v[244:247], v[84:87], v[124:127], v[244:247]\n" \
  "v_mfma_f32_16x16x32_bf16 v[240:243], v[80:83], v[124:127], v[240:243]\n" \
  "s_cbranch_scc1 1b\n" \
  "s_waitcnt lgkmcnt(0)\n" \
  "s_nop 7\n" \
  "s_nop 1\n" \
  "s_branch 5f\n" \
  "3:\n" \
  "s_waitcnt vmcnt(0)\n" \
  "s_mov_b64 exec, 1\n" \
  "global_store_dwordx2 %[xo], %[tag], %[df] sc1\n" \
  "s_mov_b64 exec, -1\n" \
  "s_branch 4b\n" \
  "5:\n"
#define ASM_B \
  "s_waitcnt lgkmcnt(0)\n" \
  "ds_read_b64_tr_b16 v[32:33], %[b0] offset:0\n" \
  "ds_read_b64_tr_b16 v[34:35], %[b0] offset:1024\n" \
  "ds_read_b128 v[48:51], %[a0] offset:0\n" \
  "ds_read_b64_tr_b16 v[36:37], %[b1] offset:0\n" \
  "ds_read_b64_tr_b16 v[38:39], %[b1] offset:1024\n" \
  "ds_read_b64_tr_b16 v[40:41], %[b2] offset:0\n" \
  "ds_read_b64_tr_b16 v[42:43], %[b2] offset:1024\n" \
  "ds_read_b64_tr_b16 v[44:45], %[b3] offset:0\n" \
  "ds_read_b64_tr_b16 v[46:47], %[b3] offset:1024\n" \
  "ds_read_b128 v[52:55], %[a0] offset:2048\n" \
  "ds_read_b128 v[56:59], %[a0] offset:4096\n" \
  "ds_read_b128 v[60:63], %[a0] offset:6144\n" \
  "ds_read_b128 v[64:67], %[a0] offset:16384\n" \
  "ds_read_b128 v[68:71], %[a0] offset:18432\n" \
  "ds_read_b128 v[72:75], %[a0] offset:20480\n" \
  "ds_read_b128 v[76:79], %[a0] offset:22528\n" \
  "s_waitcnt lgkmcnt(13)\n" \
  "v_mfma_f32_16x16x32_bf16 v[128:131], v[48:51], v[32:35], 0\n" \
  "ds_read_b64_tr_b16 v[80:81], %[b0] offset:8192\n" \
  "s_waitcnt lgkmcnt(12)\n" \
  "v_mfma_f32_16x16x32_bf16 v[132:135], v[48:51], v[36:39], 0\n" \
  "ds_read_b64_tr_b16 v[82:83], %[b0] offset:9216\n" \
  "s_waitcnt lgkmcnt(11)\n" \
  "v_mfma_f32_16x16x32_bf16 v[136:139], v[48:51], v[40:43], 0\n" \
  "ds_read_b128 v[96:99], %[a1] offset:0\n" \
  "s_add_u32 %[slot], %[slot], 1\n" \
  "s_cmp_eq_u32 %[slot], 3\n" \
  "s_waitcnt lgkmcnt(10)\n" \
  "v_mfma_f32_16x16x32_bf16 v[140:143], v[48:51], v[44:47], 0\n" \
  "ds_read_b64_tr_b16 v[84:85], %[b1] offset:8192\n" \
  "s_cselect_b32 %[dA], %[kW], %[kS]\n" \
  "s_cselect_b32 %[slot], 0, %[slot]\n" \
  "s_waitcnt lgkmcnt(10)\n" \
  "v_mfma_f32_16x16x32_bf16 v[156:159], v[52:55], v[44:47], 0\n" \
  "ds_read_b64_tr_b16 v[86:87], %[b1] offset:9216\n" \
  "s_add_u32 %[slb], %[slb], 1\n" \
  "s_cmp_eq_u32 %[slb], 4\n" \
  "v_mfma_f32_16x16x32_bf16 v[152:155], v[52:55], v[40:43], 0\n" \
  "ds_read_b64_tr_b16 v[88:89], %[b2] offset:8192\n" \
  "s_cselect_b32 %[dB], %[kWB], %[kSB]\n" \
  "s_cselect_b32 %[slb], 0, %[slb]\n" \
  "v_mfma_f32_16x16x32_bf16 v[148:151], v[52:55], v[36:39], 0\n" \
  "ds_read_b64_tr_b16 v[90:91], %[b2] offset:9216\n" \
  "v_mfma_f32_16x16x32_bf16 v[144:147], v[52:55], v[32:35], 0\n" \
  "ds_read_b64_tr_b16 v[92:93], %[b3] offset:8192\n" \
  "s_waitcnt lgkmcnt(13)\n" \
  "v_mfma_f32_16x16x32_bf16 v[160:163], v[56:59], v[32:35], 0\n" \
  "ds_read_b64_tr_b16 v[94:95], %[b3] offset:9216\n" \
  "v_mfma_f32_16x16x32_bf16 v[164:167], v[56:59], v[36:39], 0\n" \
  "ds_read_b128 v[100:103], %[a1] offset:2048\n" \
  "v_mfma_f32_16x16x32_bf16 v[168:171], v[56:59], v[40:43], 0\n" \
  "ds_read_b128 v[104:107], %[a1] offset:4096\n" \
  "v_mfma_f32_16x16x32_bf16 v[172:175], v[56:59], v[44:47], 0\n" \
  "ds_read_b128 v[108:111], %[a1] offset:6144\n" \
  "s_waitcnt lgkmcnt(15)\n" \
  "v_mfma_f32_16x16x32_bf16 v[188:191], v[60:63], v[44:47], 0\n" \
  "ds_read_b128 v[112:115], %[a1] offset:16384\n" \
  "v_mfma_f32_16x16x32_bf16 v[184:187], v[60:63], v[40:43], 0\n" \
  "ds_read_b128 v[116:119], %[a1] offset:18432\n" \
  "v_mfma_f32_16x16x32_bf16 v[180:183], v[60:63], v[36:39], 0\n" \
  "ds_read_b128 v[120:123], %[a1] offset:20480\n" \
  "v_mfma_f32_16x16x32_bf16 v[176:179], v[60:63], v[32:35], 0\n" \
  "ds_read_b128 v[124:127], %[a1] offset:22528\n" \
  "v_mfma_f32_16x16x32_bf16 v[192:195], v[64:67], v[32:35], 0\n" \
  "v_add_u32 %[b0], %[dB], %[b0]\n" \
  "v_mfma_f32_16x16x32_bf16 v[196:199], v[64:67], v[36:39], 0\n" \
  "v_add_u32 %[b1], %[dB], %[b1]\n" \
  "v_mfma_f32_16x16x32_bf16 v[200:203], v[64:67], v[40:43], 0\n" \
  "v_add_u32 %[b2], %[dB], %[b2]\n" \
  "v_mfma_f32_16x16x32_bf16 v[204:207], v[64:67], v[44:47], 0\n" \
  "v_add_u32 %[b3], %[dB], %[b3]\n" \
  "s_waitcnt lgkmcnt(15)\n" \
  "v_mfma_f32_16x16x32_bf16 v[220:223], v[68:71], v[44:47], 0\n" \
  "v_add_u32 %[a0], %[dA], %[a0]\n" \
  "v_mfma_f32_16x16x32_bf16 v[216:219], v[68:71], v[40:43], 0\n" \
  "v_add_u32 %[a1], %[dA], %[a1]\n" \
  "v_mfma_f32_16x16x32_bf16 v[212:215], v[68:71], v[36:39], 0\n" \
  "v_mfma_f32_16x16x32_bf16 v[208:211], v[68:71], v[32:35], 0\n" \
  "v_mfma_f32_16x16x32_bf16 v[224:227], v[72:75], v[32:35], 0\n" \
  "v_mfma_f32_16x16x32_bf16 v[228:231], v[72:75], v[36:39], 0\n" \
  "v_mfma_f32_16x16x32_bf16 v[232:235], v[72:75], v[40:43], 0\n" \
  "v_mfma_f32_16x16x32_bf16 v[236:239], v[72:75], v[44:47], 0\n" \
  "v_mfma_f32_16x16x32_bf16 v[252:255], v[76:79], v[44:47], 0\n" \
  "v_mfma_f32_16x16x32_bf16 v[248:251], v[76:79], v[40:43], 0\n" \
  "v_mfma_f32_16x16x32_bf16 v[244:247], v[76:79], v[36:39], 0\n" \
  "v_mfma_f32_16x16x32_bf16 v[240:243], v[76:79], v[32:35], 0\n" \
  "s_branch 2f\n" \
  ".p2align 6\n" \
  "s_nop 0\n" \
  "s_nop 0\n" \
  "s_nop 0\n" \
  "s_nop 0\n" \
  "s_nop 0\n" \
  "s_nop 0\n" \
  "s_nop 0\n" \
  "s_nop 0\n" \
  "s_nop 0\n" \
  "s_nop 0\n" \
  "1:\n" \
  "s_waitcnt lgkmcnt(13)\n" \
  "v_mfma_f32_16x16x32_bf16 v[128:131], v[48:51], v[32:35], v[128:131]\n" \
  "ds_read_b64_tr_b16 v[80:81], %[b0] offset:8192\n" \
  "s_waitcnt lgkmcnt(12)\n" \
  "v_mfma_f32_16x16x32_bf16 v[132:135], v[48:51], v[36:39], v[132:135]\n" \
  "ds_read_b64_tr_b16 v[82:83], %[b0] offset:9216\n" \
  "s_waitcnt lgkmcnt(11)\n" \
  "v_mfma_f32_16x16x32_bf16 v[136:139], v[48:51], v[40:43], v[136:139]\n" \
  "ds_read_b128 v[96:99], %[a1] offset:0\n" \
  "s_add_u32 %[slot], %[slot], 1\n" \
  "s_cmp_eq_u32 %[slot], 3\n" \
  "s_waitcnt lgkmcnt(10)\n" \
  "v_mfma_f32_16x16x32_bf16 v[140:143], v[48:51], v[44:47], v[140:143]\n" \
  "ds_read_b64_tr_b16 v[84:85], %[b1] offset:8192\n" \
  "s_cselect_b32 %[dA], %[kW], %[kS]\n" \
  "s_cselect_b32 %[slot], 0, %[slot]\n" \
  "s_waitcnt lgkmcnt(10)\n" \
  "v_mfma_f32_16x16x32_bf16 v[156:159], v[52:55], v[44:47], v[156:159]\n" \
  "ds_read_b64_tr_b16 v[86:87], %[b1] offset:9216\n" \
  "s_add_u32 %[slb], %[slb], 1\n" \
  "s_cmp_eq_u32 %[slb], 4\n" \
  "v_mfma_f32_16x16x32_bf16 v[152:155], v[52:55], v[40:43], v[152:155]\n" \
  "ds_read_b64_tr_b16 v[88:89], %[b2] offset:8192\n" \
  "s_cselect_b32 %[dB], %[kWB], %[kSB]\n" \
  "s_cselect_b32 %[slb], 0, %[slb]\n" \
  "v_mfma_f32_16x16x32_bf16 v[148:151], v[52:55], v[36:39], v[148:151]\n" \
  "ds_read_b64_tr_b16 v[90:91], %[b2] offset:9216\n" \
  "v_mfma_f32_16x16x32_bf16 v[144:147], v[52:55], v[32:35], v[144:147]\n" \
  "ds_read_b64_tr_b16 v[92:93], %[b3] offset:8192\n" \
  "s_waitcnt lgkmcnt(13)\n" \
  "v_mfma_f32_16x16x32_bf16 v[160:163], v[56:59], v[32:35], v[160:163]\n" \
  "ds_read_b64_tr_b16 v[94:95], %[b3] offset:9216\n" \
  "v_mfma_f32_16x16x32_bf16 v[164:167], v[56:59], v[36:39], v[164:167]\n" \
  "ds_read_b128 v[100:103], %[a1] offset:2048\n" \
  "v_mfma_f32_16x16x32_bf16 v[168:171], v[56:59], v[40:43], v[168:171]\n" \
  "ds_read_b128 v[104:107], %[a1] offset:4096\n" \
  "v_mfma_f32_16x16x32_bf16 v[172:175], v[56:59], v[44:47], v[172:175]\n" \
  "ds_read_b128 v[108:111], %[a1] offset:6144\n" \
  "s_waitcnt lgkmcnt(15)\n" \
  "v_mfma_f32_16x16x32_bf16 v[188:191], v[60:63], v[44:47], v[188:191]\n" \
  "ds_read_b128 v[112:115], %[a1] offset:16384\n" \
  "v_mfma_f32_16x16x32_bf16 v[184:187], v[60:63], v[40:43], v[184:187]\n" \
  "ds_read_b128 v[116:119], %[a1] offset:18432\n" \
  "v_mfma_f32_16x16x32_bf16 v[180:183], v[60:63], v[36:39], v[180:183]\n" \
  "ds_read_b128 v[120:123], %[a1] offset:20480\n" \
  "v_mfma_f32_16x16x32_bf16 v[176:179], v[60:63], v[32:35], v[176:179]\n" \
  "ds_read_b128 v[124:127], %[a1] offset:22528\n" \
  "v_mfma_f32_16x16x32_bf16 v[192:195], v[64:67], v[32:35], v[192:195]\n" \
  "v_add_u32 %[b0], %[dB], %[b0]\n" \
  "v_mfma_f32_16x16x32_bf16 v[196:199], v[64:67], v[36:39], v[196:199]\n" \
  "v_add_u32 %[b1], %[dB], %[b1]\n" \
  "v_mfma_f32_16x16x32_bf16 v[200:203], v[64:67], v[40:43], v[200:203]\n" \
  "v_add_u32 %[b2], %[dB], %[b2]\n" \
  "v_mfma_f32_16x16x32_bf16 v[204:207], v[64:67], v[44:47], v[204:207]\n" \
  "v_add_u32 %[b3], %[dB], %[b3]\n" \
  "s_waitcnt lgkmcnt(15)\n" \
  "v_mfma_f32_16x16x32_bf16 v[220:223], v[68:71], v[44:47], v[220:223]\n" \
  "v_add_u32 %[a0], %[dA], %[a0]\n" \
  "v_mfma_f32_16x16x32_bf16 v[216:219], v[68:71], v[40:43], v[216:219]\n" \
  "v_add_u32 %[a1], %[dA], %[a1]\n" \
  "v_mfma_f32_16x16x32_bf16 v[212:215], v[68:71], v[36:39], v[212:215]\n" \
  "v_mfma_f32_16x16x32_bf16 v[208:211], v[68:71], v[32:35], v[208:211]\n" \
  "v_mfma_f32_16x16x32_bf16 v[224:227], v[72:75], v[32:35], v[224:227]\n" \
  "v_mfma_f32_16x16x32_bf16 v[228:231], v[72:75], v[36:39], v[228:231]\n" \
  "v_mfma_f32_16x16x32_bf16 v[232:235], v[72:75], v[40:43], v[232:235]\n" \
  "v_mfma_f32_16x16x32_bf16 v[236:239], v[72:75], v[44:47], v[236:239]\n" \
  "v_mfma_f32_16x16x32_bf16 v[252:255], v[76:79], v[44:47], v[252:255]\n" \
  "v_mfma_f32_16x16x32_bf16 v[248:251], v[76:79], v[40:43], v[248:251]\n" \
  "v_mfma_f32_16x16x32_bf16 v[244:247], v[76:79], v[36:39], v[244:247]\n" \
  "v_mfma_f32_16x16x32_bf16 v[240:243], v[76:79], v[32:35], v[240:243]\n" \
  "2:\n" \
  "s_waitcnt lgkmcnt(13)\n" \
  "v_mfma_f32_16x16x32_bf16 v[128:131], v[96:99], v[80:83], v[128:131]\n" \
  "s_waitcnt lgkmcnt(11)\n" \
  "v_mfma_f32_16x16x32_bf16 v[132:135], v[96:99], v[84:87], v[132:135]\n" \
  "s_waitcnt lgkmcnt(9)\n" \
  "v_mfma_f32_16x16x32_bf16 v[136:139], v[96:99], v[88:91], v[136:139]\n" \
  "s_waitcnt lgkmcnt(7)\n" \
  "v_mfma_f32_16x16x32_bf16 v[140:143], v[96:99], v[92:95], v[140:143]\n" \
  "s_waitcnt lgkmcnt(6)\n" \
  "v_mfma_f32_16x16x32_bf16 v[156:159], v[100:103], v[92:95], v[156:159]\n" \
  "v_mfma_f32_16x16x32_bf16 v[152:155], v[100:103], v[88:91], v[152:155]\n" \
  "v_mfma_f32_16x16x32_bf16 v[148:151], v[100:103], v[84:87], v[148:151]\n" \
  "v_mfma_f32_16x16x32_bf16 v[144:147], v[100:103], v[80:83], v[144:147]\n" \
  "s_waitcnt lgkmcnt(0)\n" \
  "s_barrier\n" \
  "v_mfma_f32_16x16x32_bf16 v[160:163], v[104:107], v[80:83], v[160:163]\n" \
  "ds_read_b64_tr_b16 v[32:33], %[b0] offset:0\n" \
  "v_mfma_f32_16x16x32_bf16 v[164:167], v[104:107], v[84:87], v[164:167]\n" \
  "ds_read_b64_tr_b16 v[34:35], %[b0] offset:1024\n" \
  "v_mfma_f32_16x16x32_bf16 v[168:171], v[104:107], v[88:91], v[168:171]\n" \
  "ds_read_b128 v[48:51], %[a0] offset:0\n" \
  "v_mfma_f32_16x16x32_bf16 v[172:175], v[104:107], v[92:95], v[172:175]\n" \
  "ds_read_b64_tr_b16 v[36:37], %[b1] offset:0\n" \
  "v_mfma_f32_16x16x32_bf16 v[188:191], v[108:111], v[92:95], v[188:191]\n" \
  "ds_read_b64_tr_b16 v[38:39], %[b1] offset:1024\n" \
  "v_mfma_f32_16x16x32_bf16 v[184:187], v[108:111], v[88:91], v[184:187]\n" \
  "ds_read_b64_tr_b16 v[40:41], %[b2] offset:0\n" \
  "v_mfma_f32_16x16x32_bf16 v[180:183], v[108:111], v[84:87], v[180:183]\n" \
  "ds_read_b64_tr_b16 v[42:43], %[b2] offset:1024\n" \
  "v_mfma_f32_16x16x32_bf16 v[176:179], v[108:111], v[80:83], v[176:179]\n" \
  "ds_read_b64_tr_b16 v[44:45], %[b3] offset:0\n" \
  "v_mfma_f32_16x16x32_bf16 v[192:195], v[112:115], v[80:83], v[192:195]\n" \
  "ds_read_b64_tr_b16 v[46:47], %[b3] offset:1024\n" \
  "v_mfma_f32_16x16x32_bf16 v[196:199], v[112:115], v[84:87], v[196:199]\n" \
  "ds_read_b128 v[52:55], %[a0] offset:2048\n" \
  "v_mfma_f32_16x16x32_bf16 v[200:203], v[112:115], v[88:91], v[200:203]\n" \
  "ds_read_b128 v[56:59], %[a0] offset:4096\n" \
  "v_mfma_f32_16x16x32_bf16 v[204:207], v[112:115], v[92:95], v[204:207]\n" \
  "ds_read_b128 v[60:63], %[a0] offset:6144\n" \
  "v_mfma_f32_16x16x32_bf16 v[220:223], v[116:119], v[92:95], v[220:223]\n" \
  "ds_read_b128 v[64:67], %[a0] offset:16384\n" \
  "v_mfma_f32_16x16x32_bf16 v[216:219], v[116:119], v[88:91], v[216:219]\n" \
  "ds_read_b128 v[68:71], %[a0] offset:18432\n" \
  "v_mfma_f32_16x16x32_bf16 v[212:215], v[116:119], v[84:87], v[212:215]\n" \
  "ds_read_b128 v[72:75], %[a0] offset:20480\n" \
  "v_mfma_f32_16x16x32_bf16 v[208:211], v[116:119], v[80:83], v[208:211]\n" \
  "ds_read_b128 v[76:79], %[a0] offset:22528\n" \
  "v_mfma_f32_16x16x32_bf16 v[224:227], v[120:123], v[80:83], v[224:227]\n" \
  "v_mfma_f32_16x16x32_bf16 v[228:231], v[120:123], v[84:87], v[228:231]\n" \
  "s_cmp_eq_u32 %[cnt], 24\n" \
  "s_cbranch_scc1 3f\n" \
  "4:\n" \
  "v_mfma_f32_16x16x32_bf16 v[232:235], v[120:123], v[88:91], v[232:235]\n" \
  "v_mfma_f32_16x16x32_bf16 v[236:239], v[120:123], v[92:95], v[236:239]\n" \
  "v_mfma_f32_16x16x32_bf16 v[252:255], v[124:127], v[92:95], v[252:255]\n" \
  "s_sub_u32 %[cnt], %[cnt], 1\n" \
  "s_cmp_lg_u32 %[cnt], 0\n" \
  "v_mfma_f32_16x16x32_bf16 v[248:251], v[124:127], v[88:91], v[248:251]\n" \
  "v_mfma_f32_16x16x32_bf16 v[244:247], v[124:127], v[84:87], v[244:247]\n" \
  "v_mfma_f32_16x16x32_bf16 v[240:243], v[124:127], v[80:83], v[240:243]\n" \
  "s_cbranch_scc1 1b\n" \
  "s_waitcnt lgkmcnt(13)\n" \
  "v_mfma_f32_16x16x32_bf16 v[128:131], v[48:51], v[32:35], v[128:131]\n" \
  "ds_read_b64_tr_b16 v[80:81], %[nB0] offset:8192\n" \
  "v_cvt_pk_bf16_f32 v192, v192, v193\n" \
  "s_waitcnt lgkmcnt(12)\n" \
  "v_mfma_f32_16x16x32_bf16 v[132:135], v[48:51], v[36:39], v[132:135]\n" \
  "ds_read_b64_tr_b16 v[82:83], %[nB0] offset:9216\n" \
  "v_cvt_pk_bf16_f32 v193, v194, v195\n" \
  "s_waitcnt lgkmcnt(11)\n" \
  "v_mfma_f32_16x16x32_bf16 v[136:139], v[48:51], v[40:43], v[136:139]\n" \
  "ds_read_b128 v[96:99], %[nA1] offset:0\n" \
  "v_cvt_pk_bf16_f32 v194, v208, v209\n" \
  "s_waitcnt lgkmcnt(10)\n" \
  "v_mfma_f32_16x16x32_bf16 v[140:143], v[48:51], v[44:47], v[140:143]\n" \
  "ds_read_b64_tr_b16 v[84:85], %[nB1] offset:8192\n" \
  "v_cvt_pk_bf16_f32 v195, v210, v211\n" \
  "global_store_dwordx4 %[xo], v[192:195], %[xs] offset:-4096 sc1\n" \
  "s_waitcnt lgkmcnt(10)\n" \
  "v_mfma_f32_16x16x32_bf16 v[156:159], v[52:55], v[44:47], v[156:159]\n" \
  "ds_read_b64_tr_b16 v[86:87], %[nB1] offset:9216\n" \
  "v_cvt_pk_bf16_f32 v196, v196, v197\n" \
  "v_mfma_f32_16x16x32_bf16 v[152:155], v[52:55], v[40:43], v[152:155]\n" \
  "ds_read_b64_tr_b16 v[88:89], %[nB2] offset:8192\n" \
  "v_cvt_pk_bf16_f32 v197, v198, v199\n" \
  "v_mfma_f32_16x16x32_bf16 v[148:151], v[52:55], v[36:39], v[148:151]\n" \
  "ds_read_b64_tr_b16 v[90:91], %[nB2] offset:9216\n" \
  "v_cvt_pk_bf16_f32 v198, v212, v213\n" \
  "v_mfma_f32_16x16x32_bf16 v[144:147], v[52:55], v[32:35], v[144:147]\n" \
  "ds_read_b64_tr_b16 v[92:93], %[nB3] offset:8192\n" \
  "v_cvt_pk_bf16_f32 v199, v214, v215\n" \
  "global_store_dwordx4 %[xo], v[196:199], %[xs] offset:-3072 sc1\n" \
  "s_waitcnt lgkmcnt(13)\n" \
  "v_mfma_f32_16x16x32_bf16 v[160:163], v[56:59], v[32:35], v[160:163]\n" \
  "ds_read_b64_tr_b16 v[94:95], %[nB3] offset:9216\n" \
  "v_cvt_pk_bf16_f32 v200, v200, v201\n" \
  "v_mfma_f32_16x16x32_bf16 v[164:167], v[56:59], v[36:39], v[164:167]\n" \
  "ds_read_b128 v[100:103], %[nA1] offset:2048\n" \
  "v_cvt_pk_bf16_f32 v201, v202, v203\n" \
  "v_mfma_f32_16x16x32_bf16 v[168:171], v[56:59], v[40:43], v[168:171]\n" \
  "ds_read_b128 v[104:107], %[nA1] offset:4096\n" \
  "v_cvt_pk_bf16_f32 v202, v216, v217\n" \
  "v_mfma_f32_16x16x32_bf16 v[172:175], v[56:59], v[44:47], v[172:175]\n" \
  "ds_read_b128 v[108:111], %[nA1] offset:6144\n" \
  "v_cvt_pk_bf16_f32 v203, v218, v219\n" \
  "global_store_dwordx4 %[xo], v[200:203], %[xs] offset:-2048 sc1\n" \
  "s_waitcnt lgkmcnt(15)\n" \
  "v_mfma_f32_16x16x32_bf16 v[188:191], v[60:63], v[44:47], v[188:191]\n" \
  "v_cvt_pk_bf16_f32 v204, v204, v205\n" \
  "v_mfma_f32_16x16x32_bf16 v[184:187], v[60:63], v[40:43], v[184:187]\n" \
  "v_cvt_pk_bf16_f32 v205, v206, v207\n" \
  "v_mfma_f32_16x16x32_bf16 v[180:183], v[60:63], v[36:39], v[180:183]\n" \
  "v_cvt_pk_bf16_f32 v206, v220, v221\n" \
  "v_mfma_f32_16x16x32_bf16 v[176:179], v[60:63], v[32:35], v[176:179]\n" \
  "v_cvt_pk_bf16_f32 v207, v222, v223\n" \
  "global_store_dwordx4 %[xo], v[204:207], %[xs] offset:-1024 sc1\n" \
  "s_waitcnt lgkmcnt(9)\n" \
  "v_mfma_f32_16x16x32_bf16 v[128:131], v[96:99], v[80:83], v[128:131]\n" \
  "v_cvt_pk_bf16_f32 v224, v224, v225\n" \
  "s_waitcnt lgkmcnt(7)\n" \
  "v_mfma_f32_16x16x32_bf16 v[132:135], v[96:99], v[84:87], v[132:135]\n" \
  "v_cvt_pk_bf16_f32 v225, v226, v227\n" \
  "s_waitcnt lgkmcnt(5)\n" \
  "v_mfma_f32_16x16x32_bf16 v[136:139], v[96:99], v[88:91], v[136:139]\n" \
  "v_cvt_pk_bf16_f32 v226, v240, v241\n" \
  "s_waitcnt lgkmcnt(3)\n" \
  "v_mfma_f32_16x16x32_bf16 v[140:143], v[96:99], v[92:95], v[140:143]\n" \
  "s_waitcnt lgkmcnt(0)\n" \
  "s_barrier\n" \
  "v_cvt_pk_bf16_f32 v227, v242, v243\n" \
  "global_store_dwordx4 %[xo], v[224:227], %[xs] offset:0 sc1\n" \
  "v_mfma_f32_16x16x32_bf16 v[156:159], v[100:103], v[92:95], v[156:159]\n" \
  "ds_read_b64_tr_b16 v[32:33], %[nB0] offset:16384\n" \
  "v_cvt_pk_bf16_f32 v228, v228, v229\n" \
  "v_mfma_f32_16x16x32_bf16 v[152:155], v[100:103], v[88:91], v[152:155]\n" \
  "ds_read_b64_tr_b16 v[34:35], %[nB0] offset:17408\n" \
  "v_cvt_pk_bf16_f32 v229, v230, v231\n" \
  "v_mfma_f32_16x16x32_bf16 v[148:151], v[100:103], v[84:87], v[148:151]\n" \
  "ds_read_b128 v[48:51], %[nA0] offset:32768\n" \
  "v_cvt_pk_bf16_f32 v230, v244, v245\n" \
  "v_mfma_f32_16x16x32_bf16 v[144:147], v[100:103], v[80:83], v[144:147]\n" \
  "ds_read_b64_tr_b16 v[36:37], %[nB1] offset:16384\n" \
  "v_cvt_pk_bf16_f32 v231, v246, v247\n" \
  "global_store_dwordx4 %[xo], v[228:231], %[xs] offset:1024 sc1\n" \
  "v_mfma_f32_16x16x32_bf16 v[160:163], v[104:107], v[80:83], v[160:163]\n" \
  "ds_read_b64_tr_b16 v[38:39], %[nB1] offset:17408\n" \
  "v_cvt_pk_bf16_f32 v232, v232, v233\n" \
  "v_mfma_f32_16x16x32_bf16 v[164:167], v[104:107], v[84:87], v[164:167]\n" \
  "ds_read_b64_tr_b16 v[40:41], %[nB2] offset:16384\n" \
  "v_cvt_pk_bf16_f32 v233, v234, v235\n" \
  "v_mfma_f32_16x16x32_bf16 v[168:171], v[104:107], v[88:91], v[168:171]\n" \
  "ds_read_b64_tr_b16 v[42:43], %[nB2] offset:17408\n" \
  "v_cvt_pk_bf16_f32 v234, v248, v249\n" \
  "v_mfma_f32_16x16x32_bf16 v[172:175], v[104:107], v[92:95], v[172:175]\n" \
  "ds_read_b64_tr_b16 v[44:45], %[nB3] offset:16384\n" \
  "v_cvt_pk_bf16_f32 v235, v250, v251\n" \
  "global_store_dwordx4 %[xo], v[232:235], %[xs] offset:2048 sc1\n" \
  "v_mfma_f32_16x16x32_bf16 v[188:191], v[108:111], v[92:95], v[188:191]\n" \
  "ds_read_b64_tr_b16 v[46:47], %[nB3] offset:17408\n" \
  "v_cvt_pk_bf16_f32 v236, v236, v237\n" \
  "v_mfma_f32_16x16x32_bf16 v[184:187], v[108:111], v[88:91], v[184:187]\n" \
  "ds_read_b128 v[52:55], %[nA0] offset:34816\n" \
  "v_cvt_pk_bf16_f32 v237, v238, v239\n" \
  "v_mfma_f32_16x16x32_bf16 v[180:183], v[108:111], v[84:87], v[180:183]\n" \
  "ds_read_b128 v[56:59], %[nA0] offset:36864\n" \
  "v_cvt_pk_bf16_f32 v238, v252, v253\n" \
  "v_mfma_f32_16x16x32_bf16 v[176:179], v[108:111], v[80:83], v[176:179]\n" \
  "ds_read_b128 v[60:63], %[nA0] offset:38912\n" \
  "v_cvt_pk_bf16_f32 v239, v254, v255\n" \
  "global_store_dwordx4 %[xo], v[236:239], %[xs] offset:3072 sc1\n" \
  "s_waitcnt lgkmcnt(9)\n" \
  "v_mfma_f32_16x16x32_bf16 v[128:131], v[48:51], v[32:35], v[128:131]\n" \
  "ds_read_b64_tr_b16 v[80:81], %[nB0] offset:24576\n" \
  "s_waitcnt lgkmcnt(8)\n" \
  "v_mfma_f32_16x16x32_bf16 v[132:135], v[48:51], v[36:39], v[132:135]\n" \
  "ds_read_b64_tr_b16 v[82:83], %[nB0] offset:25600\n" \
  "s_waitcnt lgkmcnt(7)\n" \
  "v_mfma_f32_16x16x32_bf16 v[136:139], v[48:51], v[40:43], v[136:139]\n" \
  "ds_read_b128 v[96:99], %[nA1] offset:32768\n" \
  "s_waitcnt lgkmcnt(6)\n" \
  "v_mfma_f32_16x16x32_bf16 v[140:143], v[48:51], v[44:47], v[140:143]\n" \
  "ds_read_b64_tr_b16 v[84:85], %[nB1] offset:24576\n" \
  "s_waitcnt lgkmcnt(6)\n" \
  "v_mfma_f32_16x16x32_bf16 v[156:159], v[52:55], v[44:47], v[156:159]\n" \
  "ds_read_b64_tr_b16 v[86:87], %[nB1] offset:25600\n" \
  "v_mfma_f32_16x16x32_bf16 v[152:155], v[52:55], v[40:43], v[152:155]\n" \
  "ds_read_b64_tr_b16 v[88:89], %[nB2] offset:24576\n" \
  "v_mfma_f32_16x16x32_bf16 v[148:151], v[52:55], v[36:39], v[148:151]\n" \
  "ds_read_b64_tr_b16 v[90:91], %[nB2] offset:25600\n" \
  "v_mfma_f32_16x16x32_bf16 v[144:147], v[52:55], v[32:35], v[144:147]\n" \
  "ds_read_b64_tr_b16 v[92:93], %[nB3] offset:24576\n" \
  "s_waitcnt lgkmcnt(9)\n" \
  "v_mfma_f32_16x16x32_bf16 v[160:163], v[56:59], v[32:35], v[160:163]\n" \
  "ds_read_b64_tr_b16 v[94:95], %[nB3] offset:25600\n" \
  "v_mfma_f32_16x16x32_bf16 v[164:167], v[56:59], v[36:39], v[164:167]\n" \
  "ds_read_b128 v[100:103], %[nA1] offset:34816\n" \
  "v_mfma_f32_16x16x32_bf16 v[168:171], v[56:59], v[40:43], v[168:171]\n" \
  "ds_read_b128 v[104:107], %[nA1] offset:36864\n" \
  "v_mfma_f32_16x16x32_bf16 v[172:175], v[56:59], v[44:47], v[172:175]\n" \
  "ds_read_b128 v[108:111], %[nA1] offset:38912\n" \
  "s_waitcnt lgkmcnt(12)\n" \
  "v_mfma_f32_16x16x32_bf16 v[188:191], v[60:63], v[44:47], v[188:191]\n" \
  "v_mfma_f32_16x16x32_bf16 v[184:187], v[60:63], v[40:43], v[184:187]\n" \
  "v_mfma_f32_16x16x32_bf16 v[180:183], v[60:63], v[36:39], v[180:183]\n" \
  "v_mfma_f32_16x16x32_bf16 v[176:179], v[60:63], v[32:35], v[176:179]\n" \
  "s_waitcnt lgkmcnt(9)\n" \
  "v_mfma_f32_16x16x32_bf16 v[128:131], v[96:99], v[80:83], v[128:131]\n" \
  "s_waitcnt lgkmcnt(7)\n" \
  "v_mfma_f32_16x16x32_bf16 v[132:135], v[96:99], v[84:87], v[132:135]\n" \
  "s_waitcnt lgkmcnt(5)\n" \
  "v_mfma_f32_16x16x32_bf16 v[136:139], v[96:99], v[88:91], v[136:139]\n" \
  "s_waitcnt lgkmcnt(3)\n" \
  "v_mfma_f32_16x16x32_bf16 v[140:143], v[96:99], v[92:95], v[140:143]\n" \
  "s_waitcnt lgkmcnt(0)\n" \
  "s_barrier\n" \
  "v_mfma_f32_16x16x32_bf16 v[156:159], v[100:103], v[92:95], v[156:159]\n" \
  "ds_read_b64_tr_b16 v[32:33], %[nB0] offset:32768\n" \
  "v_mfma_f32_16x16x32_bf16 v[152:155], v[100:103], v[88:91], v[152:155]\n" \
  "ds_read_b64_tr_b16 v[34:35], %[nB0] offset:33792\n" \
  "v_mfma_f32_16x16x32_bf16 v[148:151], v[100:103], v[84:87], v[148:151]\n" \
  "ds_read_b128 v[48:51], %[nH0] offset:0\n" \
  "v_mfma_f32_16x16x32_bf16 v[144:147], v[100:103], v[80:83], v[144:147]\n" \
  "ds_read_b64_tr_b16 v[36:37], %[nB1] offset:32768\n" \
  "v_mfma_f32_16x16x32_bf16 v[160:163], v[104:107], v[80:83], v[160:163]\n" \
  "ds_read_b64_tr_b16 v[38:39], %[nB1] offset:33792\n" \
  "v_mfma_f32_16x16x32_bf16 v[164:167], v[104:107], v[84:87], v[164:167]\n" \
  "ds_read_b64_tr_b16 v[40:41], %[nB2] offset:32768\n" \
  "v_mfma_f32_16x16x32_bf16 v[168:171], v[104:107], v[88:91], v[168:171]\n" \
  "ds_read_b64_tr_b16 v[42:43], %[nB2] offset:33792\n" \
  "v_mfma_f32_16x16x32_bf16 v[172:175], v[104:107], v[92:95], v[172:175]\n" \
  "ds_read_b64_tr_b16 v[44:45], %[nB3] offset:32768\n" \
  "v_mfma_f32_16x16x32_bf16 v[188:191], v[108:111], v[92:95], v[188:191]\n" \
  "ds_read_b64_tr_b16 v[46:47], %[nB3] offset:33792\n" \
  "v_mfma_f32_16x16x32_bf16 v[184:187], v[108:111], v[88:91], v[184:187]\n" \
  "ds_read_b128 v[52:55], %[nH0] offset:2048\n" \
  "v_mfma_f32_16x16x32_bf16 v[180:183], v[108:111], v[84:87], v[180:183]\n" \
  "ds_read_b128 v[56:59], %[nH0] offset:4096\n" \
  "v_mfma_f32_16x16x32_bf16 v[176:179], v[108:111], v[80:83], v[176:179]\n" \
  "ds_read_b128 v[60:63], %[nH0] offset:6144\n" \
  "s_waitcnt lgkmcnt(9)\n" \
  "v_mfma_f32_16x16x32_bf16 v[128:131], v[48:51], v[32:35], v[128:131]\n" \
  "ds_read_b64_tr_b16 v[80:81], %[nB0] offset:40960\n" \
  "s_waitcnt lgkmcnt(8)\n" \
  "v_mfma_f32_16x16x32_bf16 v[132:135], v[48:51], v[36:39], v[132:135]\n" \
  "ds_read_b64_tr_b16 v[82:83], %[nB0] offset:41984\n" \
  "s_waitcnt lgkmcnt(7)\n" \
  "v_mfma_f32_16x16x32_bf16 v[136:139], v[48:51], v[40:43], v[136:139]\n" \
  "ds_read_b128 v[96:99], %[nH1] offset:0\n" \
  "s_waitcnt lgkmcnt(6)\n" \
  "v_mfma_f32_16x16x32_bf16 v[140:143], v[48:51], v[44:47], v[140:143]\n" \
  "ds_read_b64_tr_b16 v[84:85], %[nB1] offset:40960\n" \
  "s_waitcnt lgkmcnt(6)\n" \
  "v_mfma_f32_16x16x32_bf16 v[156:159], v[52:55], v[44:47], v[156:159]\n" \
  "ds_read_b64_tr_b16 v[86:87], %[nB1] offset:41984\n" \
  "v_mfma_f32_16x16x32_bf16 v[152:155], v[52:55], v[40:43], v[152:155]\n" \
  "ds_read_b64_tr_b16 v[88:89], %[nB2] offset:40960\n" \
  "v_mfma_f32_16x16x32_bf16 v[148:151], v[52:55], v[36:39], v[148:151]\n" \
  "ds_read_b64_tr_b16 v[90:91], %[nB2] offset:41984\n" \
  "v_mfma_f32_16x16x32_bf16 v[144:147], v[52:55], v[32:35], v[144:147]\n" \
  "ds_read_b64_tr_b16 v[92:93], %[nB3] offset:40960\n" \
  "s_waitcnt lgkmcnt(9)\n" \
  "v_mfma_f32_16x16x32_bf16 v[160:163], v[56:59], v[32:35], v[160:163]\n" \
  "ds_read_b64_tr_b16 v[94:95], %[nB3] offset:41984\n" \
  "v_mfma_f32_16x16x32_bf16 v[164:167], v[56:59], v[36:39], v[164:167]\n" \
  "ds_read_b128 v[100:103], %[nH1] offset:2048\n" \
  "v_mfma_f32_16x16x32_bf16 v[168:171], v[56:59], v[40:43], v[168:171]\n" \
  "ds_read_b128 v[104:107], %[nH1] offset:4096\n" \
  "v_mfma_f32_16x16x32_bf16 v[172:175], v[56:59], v[44:47], v[172:175]\n" \
  "ds_read_b128 v[108:111], %[nH1] offset:6144\n" \
  "s_waitcnt lgkmcnt(12)\n" \
  "v_mfma_f32_16x16x32_bf16 v[188:191], v[60:63], v[44:47], v[188:191]\n" \
  "s_waitcnt vmcnt(0)\n" \
  "s_mov_b64 exec, 1\n" \
  "global_store_dwordx2 %[xo], %[tag], %[xf] sc1\n" \
  "s_mov_b64 exec, -1\n" \
  "v_mfma_f32_16x16x32_bf16 v[184:187], v[60:63], v[40:43], v[184:187]\n" \
  "v_mov_b32 v112, 0\n" \
  "v_mfma_f32_16x16x32_bf16 v[180:183], v[60:63], v[36:39], v[180:183]\n" \
  "v_mov_b32 v113, 0\n" \
  "v_mfma_f32_16x16x32_bf16 v[176:179], v[60:63], v[32:35], v[176:179]\n" \
  "v_mov_b32 v114, %[d0]\n" \
  "s_waitcnt lgkmcnt(9)\n" \
  "v_mfma_f32_16x16x32_bf16 v[128:131], v[96:99], v[80:83], v[128:131]\n" \
  "v_mov_b32 v115, %[d1]\n" \
  "s_waitcnt lgkmcnt(7)\n" \
  "v_mfma_f32_16x16x32_bf16 v[132:135], v[96:99], v[84:87], v[132:135]\n" \
  "v_mov_b32 v116, 0\n" \
  "s_waitcnt lgkmcnt(5)\n" \
  "v_mfma_f32_16x16x32_bf16 v[136:139], v[96:99], v[88:91], v[136:139]\n" \
  "v_mov_b32 v117, 0\n" \
  "s_waitcnt lgkmcnt(3)\n" \
  "v_mfma_f32_16x16x32_bf16 v[140:143], v[96:99], v[92:95], v[140:143]\n" \
  "s_waitcnt lgkmcnt(0)\n" \
  "s_barrier\n" \
  "v_mfma_f32_16x16x32_bf16 v[156:159], v[100:103], v[92:95], v[156:159]\n" \
  "ds_read_b64_tr_b16 v[32:33], %[nB0] offset:49152\n" \
  "v_mfma_f32_16x16x32_bf16 v[152:155], v[100:103], v[88:91], v[152:155]\n" \
  "ds_read_b64_tr_b16 v[34:35], %[nB0] offset:50176\n" \
  "v_mfma_f32_16x16x32_bf16 v[148:151], v[100:103], v[84:87], v[148:151]\n" \
  "ds_read_b128 v[48:51], %[nA0] offset:0\n" \
  "v_mfma_f32_16x16x32_bf16 v[144:147], v[100:103], v[80:83], v[144:147]\n" \
  "ds_read_b64_tr_b16 v[36:37], %[nB1] offset:49152\n" \
  "v_mfma_f32_16x16x32_bf16 v[160:163], v[104:107], v[80:83], v[160:163]\n" \
  "ds_read_b64_tr_b16 v[38:39], %[nB1] offset:50176\n" \
  "v_mfma_f32_16x16x32_bf16 v[164:167], v[104:107], v[84:87], v[164:167]\n" \
  "ds_read_b64_tr_b16 v[40:41], %[nB2] offset:49152\n" \
  "v_mfma_f32_16x16x32_bf16 v[168:171], v[104:107], v[88:91], v[168:171]\n" \
  "ds_read_b64_tr_b16 v[42:43], %[nB2] offset:50176\n" \
  "v_mfma_f32_16x16x32_bf16 v[172:175], v[104:107], v[92:95], v[172:175]\n" \
  "ds_read_b64_tr_b16 v[44:45], %[nB3] offset:49152\n" \
  "v_mfma_f32_16x16x32_bf16 v[188:191], v[108:111], v[92:95], v[188:191]\n" \
  "ds_read_b64_tr_b16 v[46:47], %[nB3] offset:50176\n" \
  "v_mfma_f32_16x16x32_bf16 v[184:187], v[108:111], v[88:91], v[184:187]\n" \
  "ds_read_b128 v[52:55], %[nA0] offset:2048\n" \
  "v_mfma_f32_16x16x32_bf16 v[180:183], v[108:111], v[84:87], v[180:183]\n" \
  "ds_read_b128 v[56:59], %[nA0] offset:4096\n" \
  "v_mfma_f32_16x16x32_bf16 v[176:179], v[108:111], v[80:83], v[176:179]\n" \
  "ds_read_b128 v[60:63], %[nA0] offset:6144\n" \
  "s_waitcnt lgkmcnt(9)\n" \
  "v_mfma_f32_16x16x32_bf16 v[128:131], v[48:51], v[32:35], v[128:131]\n" \
  "ds_read_b64_tr_b16 v[80:81], %[nB0] offset:57344\n" \
  "s_waitcnt lgkmcnt(8)\n" \
  "v_mfma_f32_16x16x32_bf16 v[132:135], v[48:51], v[36:39], v[132:135]\n" \
  "ds_read_b64_tr_b16 v[82:83], %[nB0] offset:58368\n" \
  "s_waitcnt lgkmcnt(7)\n" \
  "v_mfma_f32_16x16x32_bf16 v[136:139], v[48:51], v[40:43], v[136:139]\n" \
  "ds_read_b128 v[96:99], %[nA1] offset:0\n" \
  "s_waitcnt lgkmcnt(6)\n" \
  "v_mfma_f32_16x16x32_bf16 v[140:143], v[48:51], v[44:47], v[140:143]\n" \
  "ds_read_b64_tr_b16 v[84:85], %[nB1] offset:57344\n" \
  "s_waitcnt lgkmcnt(6)\n" \
  "v_mfma_f32_16x16x32_bf16 v[156:159], v[52:55], v[44:47], v[156:159]\n" \
  "ds_read_b64_tr_b16 v[86:87], %[nB1] offset:58368\n" \
  "v_mfma_f32_16x16x32_bf16 v[152:155], v[52:55], v[40:43], v[152:155]\n" \
  "ds_read_b64_tr_b16 v[88:89], %[nB2] offset:57344\n" \
  "v_mfma_f32_16x16x32_bf16 v[148:151], v[52:55], v[36:39], v[148:151]\n" \
  "ds_read_b64_tr_b16 v[90:91], %[nB2] offset:58368\n" \
  "v_mfma_f32_16x16x32_bf16 v[144:147], v[52:55], v[32:35], v[144:147]\n" \
  "ds_read_b64_tr_b16 v[92:93], %[nB3] offset:57344\n" \
  "s_waitcnt lgkmcnt(9)\n" \
  "v_mfma_f32_16x16x32_bf16 v[160:163], v[56:59], v[32:35], v[160:163]\n" \
  "ds_read_b64_tr_b16 v[94:95], %[nB3] offset:58368\n" \
  "v_mfma_f32_16x16x32_bf16 v[164:167], v[56:59], v[36:39], v[164:167]\n" \
  "ds_read_b128 v[100:103], %[nA1] offset:2048\n" \
  "v_mfma_f32_16x16x32_bf16 v[168:171], v[56:59], v[40:43], v[168:171]\n" \
  "ds_read_b128 v[104:107], %[nA1] offset:4096\n" \
  "v_mfma_f32_16x16x32_bf16 v[172:175], v[56:59], v[44:47], v[172:175]\n" \
  "ds_read_b128 v[108:111], %[nA1] offset:6144\n" \
  "s_waitcnt lgkmcnt(12)\n" \
  "v_mfma_f32_16x16x32_bf16 v[188:191], v[60:63], v[44:47], v[188:191]\n" \
  "v_mfma_f32_16x16x32_bf16 v[184:187], v[60:63], v[40:43], v[184:187]\n" \
  "v_mfma_f32_16x16x32_bf16 v[180:183], v[60:63], v[36:39], v[180:183]\n" \
  "v_mfma_f32_16x16x32_bf16 v[176:179], v[60:63], v[32:35], v[176:179]\n" \
  "s_waitcnt lgkmcnt(9)\n" \
  "v_mfma_f32_16x16x32_bf16 v[128:131], v[96:99], v[80:83], v[128:131]\n" \
  "s_waitcnt lgkmcnt(7)\n" \
  "v_mfma_f32_16x16x32_bf16 v[132:135], v[96:99], v[84:87], v[132:135]\n" \
  "s_waitcnt lgkmcnt(5)\n" \
  "v_mfma_f32_16x16x32_bf16 v[136:139], v[96:99], v[88:91], v[136:139]\n" \
  "s_waitcnt lgkmcnt(3)\n" \
  "v_mfma_f32_16x16x32_bf16 v[140:143], v[96:99], v[92:95], v[140:143]\n" \
  "s_waitcnt lgkmcnt(0)\n" \
  "s_barrier\n" \
  "v_mfma_f32_16x16x32_bf16 v[156:159], v[100:103], v[92:95], v[156:159]\n" \
  "ds_read_b64_tr_b16 v[32:33], %[nB0] offset:0\n" \
  "v_mfma_f32_16x16x32_bf16 v[152:155], v[100:103], v[88:91], v[152:155]\n" \
  "ds_read_b64_tr_b16 v[34:35], %[nB0] offset:1024\n" \
  "v_mfma_f32_16x16x32_bf16 v[148:151], v[100:103], v[84:87], v[148:151]\n" \
  "ds_read_b128 v[48:51], %[nA0] offset:32768\n" \
  "v_mfma_f32_16x16x32_bf16 v[144:147], v[100:103], v[80:83], v[144:147]\n" \
  "ds_read_b64_tr_b16 v[36:37], %[nB1] offset:0\n" \
  "v_mfma_f32_16x16x32_bf16 v[160:163], v[104:107], v[80:83], v[160:163]\n" \
  "ds_read_b64_tr_b16 v[38:39], %[nB1] offset:1024\n" \
  "v_mfma_f32_16x16x32_bf16 v[164:167], v[104:107], v[84:87], v[164:167]\n" \
  "ds_read_b64_tr_b16 v[40:41], %[nB2] offset:0\n" \
  "v_mfma_f32_16x16x32_bf16 v[168:171], v[104:107], v[88:91], v[168:171]\n" \
  "ds_read_b64_tr_b16 v[42:43], %[nB2] offset:1024\n" \
  "v_mfma_f32_16x16x32_bf16 v[172:175], v[104:107], v[92:95], v[172:175]\n" \
  "ds_read_b64_tr_b16 v[44:45], %[nB3] offset:0\n" \
  "v_mfma_f32_16x16x32_bf16 v[188:191], v[108:111], v[92:95], v[188:191]\n" \
  "ds_read_b64_tr_b16 v[46:47], %[nB3] offset:1024\n" \
  "v_mfma_f32_16x16x32_bf16 v[184:187], v[108:111], v[88:91], v[184:187]\n" \
  "ds_read_b128 v[52:55], %[nA0] offset:34816\n" \
  "v_mfma_f32_16x16x32_bf16 v[180:183], v[108:111], v[84:87], v[180:183]\n" \
  "ds_read_b128 v[56:59], %[nA0] offset:36864\n" \
  "v_mfma_f32_16x16x32_bf16 v[176:179], v[108:111], v[80:83], v[176:179]\n" \
  "ds_read_b128 v[60:63], %[nA0] offset:38912\n" \
  "s_waitcnt lgkmcnt(9)\n" \
  "v_mfma_f32_16x16x32_bf16 v[128:131], v[48:51], v[32:35], v[128:131]\n" \
  "ds_read_b64_tr_b16 v[80:81], %[nB0] offset:8192\n" \
  "s_waitcnt lgkmcnt(8)\n" \
  "v_mfma_f32_16x16x32_bf16 v[132:135], v[48:51], v[36:39], v[132:135]\n" \
  "ds_read_b64_tr_b16 v[82:83], %[nB0] offset:9216\n" \
  "s_waitcnt lgkmcnt(7)\n" \
  "v_mfma_f32_16x16x32_bf16 v[136:139], v[48:51], v[40:43], v[136:139]\n" \
  "ds_read_b128 v[96:99], %[nA1] offset:32768\n" \
  "s_waitcnt lgkmcnt(6)\n" \
  "v_mfma_f32_16x16x32_bf16 v[140:143], v[48:51], v[44:47], v[140:143]\n" \
  "ds_read_b64_tr_b16 v[84:85], %[nB1] offset:8192\n" \
  "s_waitcnt lgkmcnt(6)\n" \
  "v_mfma_f32_16x16x32_bf16 v[156:159], v[52:55], v[44:47], v[156:159]\n" \
  "ds_read_b64_tr_b16 v[86:87], %[nB1] offset:9216\n" \
  "v_mfma_f32_16x16x32_bf16 v[152:155], v[52:55], v[40:43], v[152:155]\n" \
  "ds_read_b64_tr_b16 v[88:89], %[nB2] offset:8192\n" \
  "v_mfma_f32_16x16x32_bf16 v[148:151], v[52:55], v[36:39], v[148:151]\n" \
  "ds_read_b64_tr_b16 v[90:91], %[nB2] offset:9216\n" \
  "v_mfma_f32_16x16x32_bf16 v[144:147], v[52:55], v[32:35], v[144:147]\n" \
  "ds_read_b64_tr_b16 v[92:93], %[nB3] offset:8192\n" \
  "s_waitcnt lgkmcnt(9)\n" \
  "v_mfma_f32_16x16x32_bf16 v[160:163], v[56:59], v[32:35], v[160:163]\n" \
  "ds_read_b64_tr_b16 v[94:95], %[nB3] offset:9216\n" \
  "v_mfma_f32_16x16x32_bf16 v[164:167], v[56:59], v[36:39], v[164:167]\n" \
  "ds_read_b128 v[100:103], %[nA1] offset:34816\n" \
  "v_mfma_f32_16x16x32_bf16 v[168:171], v[56:59], v[40:43], v[168:171]\n" \
  "ds_read_b128 v[104:107], %[nA1] offset:36864\n" \
  "v_mfma_f32_16x16x32_bf16 v[172:175], v[56:59], v[44:47], v[172:175]\n" \
  "ds_read_b128 v[108:111], %[nA1] offset:38912\n" \
  "s_waitcnt lgkmcnt(12)\n" \
  "v_mfma_f32_16x16x32_bf16 v[188:191], v[60:63], v[44:47], v[188:191]\n" \
  "v_mfma_f32_16x16x32_bf16 v[184:187], v[60:63], v[40:43], v[184:187]\n" \
  "v_mfma_f32_16x16x32_bf16 v[180:183], v[60:63], v[36:39], v[180:183]\n" \
  "v_mfma_f32_16x16x32_bf16 v[176:179], v[60:63], v[32:35], v[176:179]\n" \
  "s_waitcnt lgkmcnt(9)\n" \
  "v_mfma_f32_16x16x32_bf16 v[128:131], v[96:99], v[80:83], v[128:131]\n" \
  "s_waitcnt lgkmcnt(7)\n" \
  "v_mfma_f32_16x16x32_bf16 v[132:135], v[96:99], v[84:87], v[132:135]\n" \
  "s_waitcnt lgkmcnt(5)\n" \
  "v_mfma_f32_16x16x32_bf16 v[136:139], v[96:99], v[88:91], v[136:139]\n" \
  "s_waitcnt lgkmcnt(3)\n" \
  "v_mfma_f32_16x16x32_bf16 v[140:143], v[96:99], v[92:95], v[140:143]\n" \
  "s_waitcnt lgkmcnt(0)\n" \
  "s_barrier\n" \
  "v_mfma_f32_16x16x32_bf16 v[156:159], v[100:103], v[92:95], v[156:159]\n" \
  "ds_read_b64_tr_b16 v[32:33], %[nB0] offset:16384\n" \
  "v_mfma_f32_16x16x32_bf16 v[152:155], v[100:103], v[88:91], v[152:155]\n" \
  "ds_read_b64_tr_b16 v[34:35], %[nB0] offset:17408\n" \
  "global_load_dwordx4 v[208:211], %[xo], %[xl] offset:-4096 sc1\n" \
  "v_mfma_f32_16x16x32_bf16 v[148:151], v[100:103], v[84:87], v[148:151]\n" \
  "ds_read_b128 v[48:51], %[nH0] offset:0\n" \
  "v_mfma_f32_16x16x32_bf16 v[144:147], v[100:103], v[80:83], v[144:147]\n" \
  "ds_read_b64_tr_b16 v[36:37], %[nB1] offset:16384\n" \
  "global_load_dwordx4 v[212:215], %[xo], %[xl] offset:-3072 sc1\n" \
  "v_mfma_f32_16x16x32_bf16 v[160:163], v[104:107], v[80:83], v[160:163]\n" \
  "ds_read_b64_tr_b16 v[38:39], %[nB1] offset:17408\n" \
  "v_mfma_f32_16x16x32_bf16 v[164:167], v[104:107], v[84:87], v[164:167]\n" \
  "ds_read_b64_tr_b16 v[40:41], %[nB2] offset:16384\n" \
  "global_load_dwordx4 v[216:219], %[xo], %[xl] offset:-2048 sc1\n" \
  "v_mfma_f32_16x16x32_bf16 v[168:171], v[104:107], v[88:91], v[168:171]\n" \
  "ds_read_b64_tr_b16 v[42:43], %[nB2] offset:17408\n" \
  "v_mfma_f32_16x16x32_bf16 v[172:175], v[104:107], v[92:95], v[172:175]\n" \
  "ds_read_b64_tr_b16 v[44:45], %[nB3] offset:16384\n" \
  "global_load_dwordx4 v[220:223], %[xo], %[xl] offset:-1024 sc1\n" \
  "v_mfma_f32_16x16x32_bf16 v[188:191], v[108:111], v[92:95], v[188:191]\n" \
  "ds_read_b64_tr_b16 v[46:47], %[nB3] offset:17408\n" \
  "v_mfma_f32_16x16x32_bf16 v[184:187], v[108:111], v[88:91], v[184:187]\n" \
  "ds_read_b128 v[52:55], %[nH0] offset:2048\n" \
  "v_mfma_f32_16x16x32_bf16 v[180:183], v[108:111], v[84:87], v[180:183]\n" \
  "ds_read_b128 v[56:59], %[nH0] offset:4096\n" \
  "v_mfma_f32_16x16x32_bf16 v[176:179], v[108:111], v[80:83], v[176:179]\n" \
  "ds_read_b128 v[60:63], %[nH0] offset:6144\n" \
  "s_waitcnt lgkmcnt(9)\n" \
  "v_mfma_f32_16x16x32_bf16 v[128:131], v[48:51], v[32:35], v[128:131]\n" \
  "ds_read_b64_tr_b16 v[80:81], %[nB0] offset:24576\n" \
  "s_waitcnt lgkmcnt(8)\n" \
  "v_mfma_f32_16x16x32_bf16 v[132:135], v[48:51], v[36:39], v[132:135]\n" \
  "ds_read_b64_tr_b16 v[82:83], %[nB0] offset:25600\n" \
  "s_waitcnt lgkmcnt(7)\n" \
  "v_mfma_f32_16x16x32_bf16 v[136:139], v[48:51], v[40:43], v[136:139]\n" \
  "ds_read_b128 v[96:99], %[nH1] offset:0\n" \
  "s_waitcnt lgkmcnt(6)\n" \
  "v_mfma_f32_16x16x32_bf16 v[140:143], v[48:51], v[44:47], v[140:143]\n" \
  "ds_read_b64_tr_b16 v[84:85], %[nB1] offset:24576\n" \
  "s_waitcnt lgkmcnt(6)\n" \
  "v_mfma_f32_16x16x32_bf16 v[156:159], v[52:55], v[44:47], v[156:159]\n" \
  "ds_read_b64_tr_b16 v[86:87], %[nB1] offset:25600\n" \
  "v_mfma_f32_16x16x32_bf16 v[152:155], v[52:55], v[40:43], v[152:155]\n" \
  "ds_read_b64_tr_b16 v[88:89], %[nB2] offset:24576\n" \
  "v_mfma_f32_16x16x32_bf16 v[148:151], v[52:55], v[36:39], v[148:151]\n" \
  "ds_read_b64_tr_b16 v[90:91], %[nB2] offset:25600\n" \
  "v_mfma_f32_16x16x32_bf16 v[144:147], v[52:55], v[32:35], v[144:147]\n" \
  "ds_read_b64_tr_b16 v[92:93], %[nB3] offset:24576\n" \
  "s_waitcnt lgkmcnt(9)\n" \
  "v_mfma_f32_16x16x32_bf16 v[160:163], v[56:59], v[32:35], v[160:163]\n" \
  "ds_read_b64_tr_b16 v[94:95], %[nB3] offset:25600\n" \
  "v_mfma_f32_16x16x32_bf16 v[164:167], v[56:59], v[36:39], v[164:167]\n" \
  "ds_read_b128 v[100:103], %[nH1] offset:2048\n" \
  "v_mfma_f32_16x16x32_bf16 v[168:171], v[56:59], v[40:43], v[168:171]\n" \
  "ds_read_b128 v[104:107], %[nH1] offset:4096\n" \
  "v_mfma_f32_16x16x32_bf16 v[172:175], v[56:59], v[44:47], v[172:175]\n" \
  "ds_read_b128 v[108:111], %[nH1] offset:6144\n" \
  "s_waitcnt lgkmcnt(12)\n" \
  "v_mfma_f32_16x16x32_bf16 v[188:191], v[60:63], v[44:47], v[188:191]\n" \
  "global_load_dwordx4 v[240:243], %[xo], %[xl] offset:0 sc1\n" \
  "v_mfma_f32_16x16x32_bf16 v[184:187], v[60:63], v[40:43], v[184:187]\n" \
  "v_mfma_f32_16x16x32_bf16 v[180:183], v[60:63], v[36:39], v[180:183]\n" \
  "global_load_dwordx4 v[244:247], %[xo], %[xl] offset:1024 sc1\n" \
  "v_mfma_f32_16x16x32_bf16 v[176:179], v[60:63], v[32:35], v[176:179]\n" \
  "s_waitcnt lgkmcnt(9)\n" \
  "v_mfma_f32_16x16x32_bf16 v[128:131], v[96:99], v[80:83], v[128:131]\n" \
  "global_load_dwordx4 v[248:251], %[xo], %[xl] offset:2048 sc1\n" \
  "s_waitcnt lgkmcnt(7)\n" \
  "v_mfma_f32_16x16x32_bf16 v[132:135], v[96:99], v[84:87], v[132:135]\n" \
  "s_waitcnt lgkmcnt(5)\n" \
  "v_mfma_f32_16x16x32_bf16 v[136:139], v[96:99], v[88:91], v[136:139]\n" \
  "global_load_dwordx4 v[252:255], %[xo], %[xl] offset:3072 sc1\n" \
  "s_waitcnt lgkmcnt(3)\n" \
  "v_mfma_f32_16x16x32_bf16 v[140:143], v[96:99], v[92:95], v[140:143]\n" \
  "s_waitcnt lgkmcnt(0)\n" \
  "s_barrier\n" \
  "v_mfma_f32_16x16x32_bf16 v[156:159], v[100:103], v[92:95], v[156:159]\n" \
  "ds_read_b64_tr_b16 v[32:33], %[nB0] offset:32768\n" \
  "v_mfma_f32_16x16x32_bf16 v[152:155], v[100:103], v[88:91], v[152:155]\n" \
  "ds_read_b64_tr_b16 v[34:35], %[nB0] offset:33792\n" \
  "v_mfma_f32_16x16x32_bf16 v[148:151], v[100:103], v[84:87], v[148:151]\n" \
  "ds_read_b128 v[48:51], %[nA0] offset:0\n" \
  "v_mfma_f32_16x16x32_bf16 v[144:147], v[100:103], v[80:83], v[144:147]\n" \
  "ds_read_b64_tr_b16 v[36:37], %[nB1] offset:32768\n" \
  "v_mfma_f32_16x16x32_bf16 v[160:163], v[104:107], v[80:83], v[160:163]\n" \
  "ds_read_b64_tr_b16 v[38:39], %[nB1] offset:33792\n" \
  "v_mfma_f32_16x16x32_bf16 v[164:167], v[104:107], v[84:87], v[164:167]\n" \
  "ds_read_b64_tr_b16 v[40:41], %[nB2] offset:32768\n" \
  "v_mfma_f32_16x16x32_bf16 v[168:171], v[104:107], v[88:91], v[168:171]\n" \
  "ds_read_b64_tr_b16 v[42:43], %[nB2] offset:33792\n" \
  "v_mfma_f32_16x16x32_bf16 v[172:175], v[104:107], v[92:95], v[172:175]\n" \
  "ds_read_b64_tr_b16 v[44:45], %[nB3] offset:32768\n" \
  "v_mfma_f32_16x16x32_bf16 v[188:191], v[108:111], v[92:95], v[188:191]\n" \
  "ds_read_b64_tr_b16 v[46:47], %[nB3] offset:33792\n" \
  "v_mfma_f32_16x16x32_bf16 v[184:187], v[108:111], v[88:91], v[184:187]\n" \
  "ds_read_b128 v[52:55], %[nA0] offset:2048\n" \
  "v_mfma_f32_16x16x32_bf16 v[180:183], v[108:111], v[84:87], v[180:183]\n" \
  "ds_read_b128 v[56:59], %[nA0] offset:4096\n" \
  "v_mfma_f32_16x16x32_bf16 v[176:179], v[108:111], v[80:83], v[176:179]\n" \
  "ds_read_b128 v[60:63], %[nA0] offset:6144\n" \
  "s_waitcnt lgkmcnt(9)\n" \
  "v_mfma_f32_16x16x32_bf16 v[128:131], v[48:51], v[32:35], v[128:131]\n" \
  "ds_read_b64_tr_b16 v[80:81], %[nB0] offset:40960\n" \
  "s_waitcnt lgkmcnt(8)\n" \
  "v_mfma_f32_16x16x32_bf16 v[132:135], v[48:51], v[36:39], v[132:135]\n" \
  "ds_read_b64_tr_b16 v[82:83], %[nB0] offset:41984\n" \
  "s_waitcnt lgkmcnt(7)\n" \
  "v_mfma_f32_16x16x32_bf16 v[136:139], v[48:51], v[40:43], v[136:139]\n" \
  "ds_read_b128 v[96:99], %[nA1] offset:0\n" \
  "s_waitcnt lgkmcnt(6)\n" \
  "v_mfma_f32_16x16x32_bf16 v[140:143], v[48:51], v[44:47], v[140:143]\n" \
  "ds_read_b64_tr_b16 v[84:85], %[nB1] offset:40960\n" \
  "s_waitcnt lgkmcnt(6)\n" \
  "v_mfma_f32_16x16x32_bf16 v[156:159], v[52:55], v[44:47], v[156:159]\n" \
  "ds_read_b64_tr_b16 v[86:87], %[nB1] offset:41984\n" \
  "v_mfma_f32_16x16x32_bf16 v[152:155], v[52:55], v[40:43], v[152:155]\n" \
  "ds_read_b64_tr_b16 v[88:89], %[nB2] offset:40960\n" \
  "v_mfma_f32_16x16x32_bf16 v[148:151], v[52:55], v[36:39], v[148:151]\n" \
  "ds_read_b64_tr_b16 v[90:91], %[nB2] offset:41984\n" \
  "v_mfma_f32_16x16x32_bf16 v[144:147], v[52:55], v[32:35], v[144:147]\n" \
  "ds_read_b64_tr_b16 v[92:93], %[nB3] offset:40960\n" \
  "s_waitcnt lgkmcnt(9)\n" \
  "v_mfma_f32_16x16x32_bf16 v[160:163], v[56:59], v[32:35], v[160:163]\n" \
  "ds_read_b64_tr_b16 v[94:95], %[nB3] offset:41984\n" \
  "v_mfma_f32_16x16x32_bf16 v[164:167], v[56:59], v[36:39], v[164:167]\n" \
  "ds_read_b128 v[100:103], %[nA1] offset:2048\n" \
  "v_mfma_f32_16x16x32_bf16 v[168:171], v[56:59], v[40:43], v[168:171]\n" \
  "ds_read_b128 v[104:107], %[nA1] offset:4096\n" \
  "v_mfma_f32_16x16x32_bf16 v[172:175], v[56:59], v[44:47], v[172:175]\n" \
  "ds_read_b128 v[108:111], %[nA1] offset:6144\n" \
  "s_waitcnt lgkmcnt(12)\n" \
  "v_mfma_f32_16x16x32_bf16 v[188:191], v[60:63], v[44:47], v[188:191]\n" \
  "v_mfma_f32_16x16x32_bf16 v[184:187], v[60:63], v[40:43], v[184:187]\n" \
  "v_mfma_f32_16x16x32_bf16 v[180:183], v[60:63], v[36:39], v[180:183]\n" \
  "v_mfma_f32_16x16x32_bf16 v[176:179], v[60:63], v[32:35], v[176:179]\n" \
  "s_waitcnt lgkmcnt(9)\n" \
  "v_mfma_f32_16x16x32_bf16 v[128:131], v[96:99], v[80:83], v[128:131]\n" \
  "s_waitcnt lgkmcnt(7)\n" \
  "v_mfma_f32_16x16x32_bf16 v[132:135], v[96:99], v[84:87], v[132:135]\n" \
  "s_waitcnt lgkmcnt(5)\n" \
  "v_mfma_f32_16x16x32_bf16 v[136:139], v[96:99], v[88:91], v[136:139]\n" \
  "s_waitcnt lgkmcnt(3)\n" \
  "v_mfma_f32_16x16x32_bf16 v[140:143], v[96:99], v[92:95], v[140:143]\n" \
  "s_waitcnt lgkmcnt(0)\n" \
  "s_barrier\n" \
  "v_mfma_f32_16x16x32_bf16 v[156:159], v[100:103], v[92:95], v[156:159]\n" \
  "ds_read_b64_tr_b16 v[32:33], %[nB0] offset:49152\n" \
  "v_mfma_f32_16x16x32_bf16 v[152:155], v[100:103], v[88:91], v[152:155]\n" \
  "ds_read_b64_tr_b16 v[34:35], %[nB0] offset:50176\n" \
  "v_mfma_f32_16x16x32_bf16 v[148:151], v[100:103], v[84:87], v[148:151]\n" \
  "ds_read_b128 v[48:51], %[nA0] offset:32768\n" \
  "v_mfma_f32_16x16x32_bf16 v[144:147], v[100:103], v[80:83], v[144:147]\n" \
  "ds_read_b64_tr_b16 v[36:37], %[nB1] offset:49152\n" \
  "v_mfma_f32_16x16x32_bf16 v[160:163], v[104:107], v[80:83], v[160:163]\n" \
  "ds_read_b64_tr_b16 v[38:39], %[nB1] offset:50176\n" \
  "v_mfma_f32_16x16x32_bf16 v[164:167], v[104:107], v[84:87], v[164:167]\n" \
  "ds_read_b64_tr_b16 v[40:41], %[nB2] offset:49152\n" \
  "v_mfma_f32_16x16x32_bf16 v[168:171], v[104:107], v[88:91], v[168:171]\n" \
  "ds_read_b64_tr_b16 v[42:43], %[nB2] offset:50176\n" \
  "v_mfma_f32_16x16x32_bf16 v[172:175], v[104:107], v[92:95], v[172:175]\n" \
  "ds_read_b64_tr_b16 v[44:45], %[nB3] offset:49152\n" \
  "v_mfma_f32_16x16x32_bf16 v[188:191], v[108:111], v[92:95], v[188:191]\n" \
  "ds_read_b64_tr_b16 v[46:47], %[nB3] offset:50176\n" \
  "v_mfma_f32_16x16x32_bf16 v[184:187], v[108:111], v[88:91], v[184:187]\n" \
  "ds_read_b128 v[52:55], %[nA0] offset:34816\n" \
  "v_mfma_f32_16x16x32_bf16 v[180:183], v[108:111], v[84:87], v[180:183]\n" \
  "ds_read_b128 v[56:59], %[nA0] offset:36864\n" \
  "v_mfma_f32_16x16x32_bf16 v[176:179], v[108:111], v[80:83], v[176:179]\n" \
  "ds_read_b128 v[60:63], %[nA0] offset:38912\n" \
  "s_waitcnt lgkmcnt(9)\n" \
  "v_mfma_f32_16x16x32_bf16 v[128:131], v[48:51], v[32:35], v[128:131]\n" \
  "ds_read_b64_tr_b16 v[80:81], %[nB0] offset:57344\n" \
  "s_waitcnt lgkmcnt(8)\n" \
  "v_mfma_f32_16x16x32_bf16 v[132:135], v[48:51], v[36:39], v[132:135]\n" \
  "ds_read_b64_tr_b16 v[82:83], %[nB0] offset:58368\n" \
  "s_waitcnt lgkmcnt(7)\n" \
  "v_mfma_f32_16x16x32_bf16 v[136:139], v[48:51], v[40:43], v[136:139]\n" \
  "ds_read_b128 v[96:99], %[nA1] offset:32768\n" \
  "s_waitcnt lgkmcnt(6)\n" \
  "v_mfma_f32_16x16x32_bf16 v[140:143], v[48:51], v[44:47], v[140:143]\n" \
  "ds_read_b64_tr_b16 v[84:85], %[nB1] offset:57344\n" \
  "s_waitcnt lgkmcnt(6)\n" \
  "v_mfma_f32_16x16x32_bf16 v[156:159], v[52:55], v[44:47], v[156:159]\n" \
  "ds_read_b64_tr_b16 v[86:87], %[nB1] offset:58368\n" \
  "v_mfma_f32_16x16x32_bf16 v[152:155], v[52:55], v[40:43], v[152:155]\n" \
  "ds_read_b64_tr_b16 v[88:89], %[nB2] offset:57344\n" \
  "v_mfma_f32_16x16x32_bf16 v[148:151], v[52:55], v[36:39], v[148:151]\n" \
  "ds_read_b64_tr_b16 v[90:91], %[nB2] offset:58368\n" \
  "v_mfma_f32_16x16x32_bf16 v[144:147], v[52:55], v[32:35], v[144:147]\n" \
  "ds_read_b64_tr_b16 v[92:93], %[nB3] offset:57344\n" \
  "s_waitcnt lgkmcnt(9)\n" \
  "v_mfma_f32_16x16x32_bf16 v[160:163], v[56:59], v[32:35], v[160:163]\n" \
  "ds_read_b64_tr_b16 v[94:95], %[nB3] offset:58368\n" \
  "v_mfma_f32_16x16x32_bf16 v[164:167], v[56:59], v[36:39], v[164:167]\n" \
  "ds_read_b128 v[100:103], %[nA1] offset:34816\n" \
  "v_mfma_f32_16x16x32_bf16 v[168:171], v[56:59], v[40:43], v[168:171]\n" \
  "ds_read_b128 v[104:107], %[nA1] offset:36864\n" \
  "v_mfma_f32_16x16x32_bf16 v[172:175], v[56:59], v[44:47], v[172:175]\n" \
  "ds_read_b128 v[108:111], %[nA1] offset:38912\n" \
  "s_waitcnt lgkmcnt(12)\n" \
  "v_mfma_f32_16x16x32_bf16 v[188:191], v[60:63], v[44:47], v[188:191]\n" \
  "v_mfma_f32_16x16x32_bf16 v[184:187], v[60:63], v[40:43], v[184:187]\n" \
  "v_mfma_f32_16x16x32_bf16 v[180:183], v[60:63], v[36:39], v[180:183]\n" \
  "v_mfma_f32_16x16x32_bf16 v[176:179], v[60:63], v[32:35], v[176:179]\n" \
  "s_waitcnt lgkmcnt(9)\n" \
  "v_mfma_f32_16x16x32_bf16 v[128:131], v[96:99], v[80:83], v[128:131]\n" \
  "s_waitcnt lgkmcnt(7)\n" \
  "v_mfma_f32_16x16x32_bf16 v[132:135], v[96:99], v[84:87], v[132:135]\n" \
  "s_waitcnt lgkmcnt(5)\n" \
  "v_mfma_f32_16x16x32_bf16 v[136:139], v[96:99], v[88:91], v[136:139]\n" \
  "s_waitcnt lgkmcnt(3)\n" \
  "v_mfma_f32_16x16x32_bf16 v[140:143], v[96:99], v[92:95], v[140:143]\n" \
  "s_waitcnt lgkmcnt(0)\n" \
  "s_barrier\n" \
  "v_mfma_f32_16x16x32_bf16 v[156:159], v[100:103], v[92:95], v[156:159]\n" \
  "v_mfma_f32_16x16x32_bf16 v[152:155], v[100:103], v[88:91], v[152:155]\n" \
  "v_mfma_f32_16x16x32_bf16 v[148:151], v[100:103], v[84:87], v[148:151]\n" \
  "v_mfma_f32_16x16x32_bf16 v[144:147], v[100:103], v[80:83], v[144:147]\n" \
  "v_mfma_f32_16x16x32_bf16 v[160:163], v[104:107], v[80:83], v[160:163]\n" \
  "v_mfma_f32_16x16x32_bf16 v[164:167], v[104:107], v[84:87], v[164:167]\n" \
  "v_mfma_f32_16x16x32_bf16 v[168:171], v[104:107], v[88:91], v[168:171]\n" \
  "v_mfma_f32_16x16x32_bf16 v[172:175], v[104:107], v[92:95], v[172:175]\n" \
  "v_mfma_f32_16x16x32_bf16 v[188:191], v[108:111], v[92:95], v[188:191]\n" \
  "v_mfma_f32_16x16x32_bf16 v[184:187], v[108:111], v[88:91], v[184:187]\n" \
  "v_mfma_f32_16x16x32_bf16 v[180:183], v[108:111], v[84:87], v[180:183]\n" \
  "v_mfma_f32_16x16x32_bf16 v[176:179], v[108:111], v[80:83], v[176:179]\n" \
  "s_waitcnt vmcnt(0)\n" \
  "v_mfma_f32_16x16x32_bf16 v[128:131], v[114:117], v[208:211], v[128:131]\n" \
  "v_mfma_f32_16x16x32_bf16 v[132:135], v[114:117], v[212:215], v[132:135]\n" \
  "v_mfma_f32_16x16x32_bf16 v[136:139], v[114:117], v[216:219], v[136:139]\n" \
  "v_mfma_f32_16x16x32_bf16 v[140:143], v[114:117], v[220:223], v[140:143]\n" \
  "v_mfma_f32_16x16x32_bf16 v[144:147], v[112:115], v[208:211], v[144:147]\n" \
  "v_mfma_f32_16x16x32_bf16 v[148:151], v[112:115], v[212:215], v[148:151]\n" \
  "v_mfma_f32_16x16x32_bf16 v[152:155], v[112:115], v[216:219], v[152:155]\n" \
  "v_mfma_f32_16x16x32_bf16 v[156:159], v[112:115], v[220:223], v[156:159]\n" \
  "v_mfma_f32_16x16x32_bf16 v[160:163], v[114:117], v[240:243], v[160:163]\n" \
  "v_mfma_f32_16x16x32_bf16 v[164:167], v[114:117], v[244:247], v[164:167]\n" \
  "v_mfma_f32_16x16x32_bf16 v[168:171], v[114:117], v[248:251], v[168:171]\n" \
  "v_mfma_f32_16x16x32_bf16 v[172:175], v[114:117], v[252:255], v[172:175]\n" \
  "v_mfma_f32_16x16x32_bf16 v[176:179], v[112:115], v[240:243], v[176:179]\n" \
  "v_mfma_f32_16x16x32_bf16 v[180:183], v[112:115], v[244:247], v[180:183]\n" \
  "v_mfma_f32_16x16x32_bf16 v[184:187], v[112:115], v[248:251], v[184:187]\n" \
  "v_mfma_f32_16x16x32_bf16 v[188:191], v[112:115], v[252:255], v[188:191]\n" \
  "s_nop 7\n" \
  "s_nop 1\n" \
  "s_branch 5f\n" \
  "3:\n" \
  "s_waitcnt vmcnt(0)\n" \
  "s_mov_b64 exec, 1\n" \
  "global_store_dwordx2 %[xo], %[tag], %[df] sc1\n" \
  "s_mov_b64 exec, -1\n" \
  "s_branch 4b\n" \
  "5:\n"
#define ASM_C \
  "s_waitcnt lgkmcnt(0)\n" \
  "ds_read_b128 v[32:35], %[a0] offset:0\n" \
  "ds_read_b128 v[48:51], %[b0] offset:0\n" \
  "ds_read_b128 v[36:39], %[a0] offset:2048\n" \
  "ds_read_b128 v[40:43], %[a0] offset:4096\n" \
  "ds_read_b128 v[44:47], %[a0] offset:6144\n" \
  "ds_read_b128 v[52:55], %[b0] offset:2080\n" \
  "ds_read_b128 v[56:59], %[b0] offset:4160\n" \
  "ds_read_b128 v[60:63], %[b0] offset:6240\n" \
  "ds_read_b128 v[64:67], %[b0] offset:8320\n" \
  "ds_read_b128 v[68:71], %[b0] offset:10400\n" \
  "ds_read_b128 v[72:75], %[b0] offset:12480\n" \
  "ds_read_b128 v[76:79], %[b0] offset:14560\n" \
  "s_waitcnt lgkmcnt(10)\n" \
  "v_mfma_f32_16x16x32_bf16 v[128:131], v[32:35], v[48:51], 0\n" \
  "ds_read_b128 v[80:83], %[a0] offset:1024\n" \
  "s_waitcnt lgkmcnt(10)\n" \
  "v_mfma_f32_16x16x32_bf16 v[132:135], v[36:39], v[48:51], 0\n" \
  "ds_read_b128 v[96:99], %[b0] offset:1024\n" \
  "s_waitcnt lgkmcnt(10)\n" \
  "v_mfma_f32_16x16x32_bf16 v[136:139], v[40:43], v[48:51], 0\n" \
  "ds_read_b128 v[84:87], %[a0] offset:3072\n" \
  "s_add_u32 %[slot], %[slot], 1\n" \
  "s_cmp_eq_u32 %[slot], 3\n" \
  "s_waitcnt lgkmcnt(10)\n" \
  "v_mfma_f32_16x16x32_bf16 v[140:143], v[44:47], v[48:51], 0\n" \
  "ds_read_b128 v[88:91], %[a0] offset:5120\n" \
  "s_cselect_b32 %[dA], %[kW], %[kS]\n" \
  "s_cselect_b32 %[dB], %[kWB], %[kSB]\n" \
  "s_cselect_b32 %[slot], 0, %[slot]\n" \
  "s_waitcnt lgkmcnt(10)\n" \
  "v_mfma_f32_16x16x32_bf16 v[156:159], v[44:47], v[52:55], 0\n" \
  "ds_read_b128 v[92:95], %[a0] offset:7168\n" \
  "v_mfma_f32_16x16x32_bf16 v[152:155], v[40:43], v[52:55], 0\n" \
  "ds_read_b128 v[100:103], %[b0] offset:3104\n" \
  "v_mfma_f32_16x16x32_bf16 v[148:151], v[36:39], v[52:55], 0\n" \
  "ds_read_b128 v[104:107], %[b0] offset:5184\n" \
  "v_mfma_f32_16x16x32_bf16 v[144:147], v[32:35], v[52:55], 0\n" \
  "ds_read_b128 v[108:111], %[b0] offset:7264\n" \
  "s_waitcnt lgkmcnt(13)\n" \
  "v_mfma_f32_16x16x32_bf16 v[160:163], v[32:35], v[56:59], 0\n" \
  "ds_read_b128 v[112:115], %[b0] offset:9344\n" \
  "v_mfma_f32_16x16x32_bf16 v[164:167], v[36:39], v[56:59], 0\n" \
  "ds_read_b128 v[116:119], %[b0] offset:11424\n" \
  "v_mfma_f32_16x16x32_bf16 v[168:171], v[40:43], v[56:59], 0\n" \
  "ds_read_b128 v[120:123], %[b0] offset:13504\n" \
  "v_mfma_f32_16x16x32_bf16 v[172:175], v[44:47], v[56:59], 0\n" \
  "ds_read_b128 v[124:127], %[b0] offset:15584\n" \
  "s_waitcnt lgkmcnt(15)\n" \
  "v_mfma_f32_16x16x32_bf16 v[188:191], v[44:47], v[60:63], 0\n" \
  "v_add_u32 %[b0], %[dB], %[b0]\n" \
  "v_mfma_f32_16x16x32_bf16 v[184:187], v[40:43], v[60:63], 0\n" \
  "v_add_u32 %[a0], %[dA], %[a0]\n" \
  "v_mfma_f32_16x16x32_bf16 v[180:183], v[36:39], v[60:63], 0\n" \
  "v_mfma_f32_16x16x32_bf16 v[176:179], v[32:35], v[60:63], 0\n" \
  "v_mfma_f32_16x16x32_bf16 v[192:195], v[32:35], v[64:67], 0\n" \
  "v_mfma_f32_16x16x32_bf16 v[196:199], v[36:39], v[64:67], 0\n" \
  "v_mfma_f32_16x16x32_bf16 v[200:203], v[40:43], v[64:67], 0\n" \
  "v_mfma_f32_16x16x32_bf16 v[204:207], v[44:47], v[64:67], 0\n" \
  "s_waitcnt lgkmcnt(14)\n" \
  "v_mfma_f32_16x16x32_bf16 v[220:223], v[44:47], v[68:71], 0\n" \
  "v_mfma_f32_16x16x32_bf16 v[216:219], v[40:43], v[68:71], 0\n" \
  "v_mfma_f32_16x16x32_bf16 v[212:215], v[36:39], v[68:71], 0\n" \
  "v_mfma_f32_16x16x32_bf16 v[208:211], v[32:35], v[68:71], 0\n" \
  "s_waitcnt lgkmcnt(13)\n" \
  "v_mfma_f32_16x16x32_bf16 v[224:227], v[32:35], v[72:75], 0\n" \
  "v_mfma_f32_16x16x32_bf16 v[228:231], v[36:39], v[72:75], 0\n" \
  "v_mfma_f32_16x16x32_bf16 v[232:235], v[40:43], v[72:75], 0\n" \
  "v_mfma_f32_16x16x32_bf16 v[236:239], v[44:47], v[72:75], 0\n" \
  "s_waitcnt lgkmcnt(12)\n" \
  "v_mfma_f32_16x16x32_bf16 v[252:255], v[44:47], v[76:79], 0\n" \
  "v_mfma_f32_16x16x32_bf16 v[248:251], v[40:43], v[76:79], 0\n" \
  "v_mfma_f32_16x16x32_bf16 v[244:247], v[36:39], v[76:79], 0\n" \
  "v_mfma_f32_16x16x32_bf16 v[240:243], v[32:35], v[76:79], 0\n" \
  "s_branch 2f\n" \
  ".p2align 6\n" \
  "s_nop 0\n" \
  "s_nop 0\n" \
  "s_nop 0\n" \
  "s_nop 0\n" \
  "s_nop 0\n" \
  "s_nop 0\n" \
  "s_nop 0\n" \
  "s_nop 0\n" \
  "s_nop 0\n" \
  "s_nop 0\n" \
  "1:\n" \
  "s_waitcnt lgkmcnt(10)\n" \
  "v_mfma_f32_16x16x32_bf16 v[128:131], v[32:35], v[48:51], v[128:131]\n" \
  "ds_read_b128 v[80:83], %[a0] offset:1024\n" \
  "s_waitcnt lgkmcnt(10)\n" \
  "v_mfma_f32_16x16x32_bf16 v[132:135], v[36:39], v[48:51], v[132:135]\n" \
  "ds_read_b128 v[96:99], %[b0] offset:1024\n" \
  "s_waitcnt lgkmcnt(10)\n" \
  "v_mfma_f32_16x16x32_bf16 v[136:139], v[40:43], v[48:51], v[136:139]\n" \
  "ds_read_b128 v[84:87], %[a0] offset:3072\n" \
  "s_add_u32 %[slot], %[slot], 1\n" \
  "s_cmp_eq_u32 %[slot], 3\n" \
  "s_waitcnt lgkmcnt(10)\n" \
  "v_mfma_f32_16x16x32_bf16 v[140:143], v[44:47], v[48:51], v[140:143]\n" \
  "ds_read_b128 v[88:91], %[a0] offset:5120\n" \
  "s_cselect_b32 %[dA], %[kW], %[kS]\n" \
  "s_cselect_b32 %[dB], %[kWB], %[kSB]\n" \
  "s_cselect_b32 %[slot], 0, %[slot]\n" \
  "s_waitcnt lgkmcnt(10)\n" \
  "v_mfma_f32_16x16x32_bf16 v[156:159], v[44:47], v[52:55], v[156:159]\n" \
  "ds_read_b128 v[92:95], %[a0] offset:7168\n" \
  "v_mfma_f32_16x16x32_bf16 v[152:155], v[40:43], v[52:55], v[152:155]\n" \
  "ds_read_b128 v[100:103], %[b0] offset:3104\n" \
  "v_mfma_f32_16x16x32_bf16 v[148:151], v[36:39], v[52:55], v[148:151]\n" \
  "ds_read_b128 v[104:107], %[b0] offset:5184\n" \
  "v_mfma_f32_16x16x32_bf16 v[144:147], v[32:35], v[52:55], v[144:147]\n" \
  "ds_read_b128 v[108:111], %[b0] offset:7264\n" \
  "s_waitcnt lgkmcnt(13)\n" \
  "v_mfma_f32_16x16x32_bf16 v[160:163], v[32:35], v[56:59], v[160:163]\n" \
  "ds_read_b128 v[112:115], %[b0] offset:9344\n" \
  "v_mfma_f32_16x16x32_bf16 v[164:167], v[36:39], v[56:59], v[164:167]\n" \
  "ds_read_b128 v[116:119], %[b0] offset:11424\n" \
  "v_mfma_f32_16x16x32_bf16 v[168:171], v[40:43], v[56:59], v[168:171]\n" \
  "ds_read_b128 v[120:123], %[b0] offset:13504\n" \
  "v_mfma_f32_16x16x32_bf16 v[172:175], v[44:47], v[56:59], v[172:175]\n" \
  "ds_read_b128 v[124:127], %[b0] offset:15584\n" \
  "s_waitcnt lgkmcnt(15)\n" \
  "v_mfma_f32_16x16x32_bf16 v[188:191], v[44:47], v[60:63], v[188:191]\n" \
  "v_add_u32 %[b0], %[dB], %[b0]\n" \
  "v_mfma_f32_16x16x32_bf16 v[184:187], v[40:43], v[60:63], v[184:187]\n" \
  "v_add_u32 %[a0], %[dA], %[a0]\n" \
  "v_mfma_f32_16x16x32_bf16 v[180:183], v[36:39], v[60:63], v[180:183]\n" \
  "v_mfma_f32_16x16x32_bf16 v[176:179], v[32:35], v[60:63], v[176:179]\n" \
  "v_mfma_f32_16x16x32_bf16 v[192:195], v[32:35], v[64:67], v[192:195]\n" \
  "v_mfma_f32_16x16x32_bf16 v[196:199], v[36:39], v[64:67], v[196:199]\n" \
  "v_mfma_f32_16x16x32_bf16 v[200:203], v[40:43], v[64:67], v[200:203]\n" \
  "v_mfma_f32_16x16x32_bf16 v[204:207], v[44:47], v[64:67], v[204:207]\n" \
  "s_waitcnt lgkmcnt(14)\n" \
  "v_mfma_f32_16x16x32_bf16 v[220:223], v[44:47], v[68:71], v[220:223]\n" \
  "v_mfma_f32_16x16x32_bf16 v[216:219], v[40:43], v[68:71], v[216:219]\n" \
  "v_mfma_f32_16x16x32_bf16 v[212:215], v[36:39], v[68:71], v[212:215]\n" \
  "v_mfma_f32_16x16x32_bf16 v[208:211], v[32:35], v[68:71], v[208:211]\n" \
  "s_waitcnt lgkmcnt(13)\n" \
  "v_mfma_f32_16x16x32_bf16 v[224:227], v[32:35], v[72:75], v[224:227]\n" \
  "v_mfma_f32_16x16x32_bf16 v[228:231], v[36:39], v[72:75], v[228:231]\n" \
  "v_mfma_f32_16x16x32_bf16 v[232:235], v[40:43], v[72:75], v[232:235]\n" \
  "v_mfma_f32_16x16x32_bf16 v[236:239], v[44:47], v[72:75], v[236:239]\n" \
  "s_waitcnt lgkmcnt(12)\n" \
  "v_mfma_f32_16x16x32_bf16 v[252:255], v[44:47], v[76:79], v[252:255]\n" \
  "v_mfma_f32_16x16x32_bf16 v[248:251], v[40:43], v[76:79], v[248:251]\n" \
  "v_mfma_f32_16x16x32_bf16 v[244:247], v[36:39], v[76:79], v[244:247]\n" \
  "v_mfma_f32_16x16x32_bf16 v[240:243], v[32:35], v[76:79], v[240:243]\n" \
  "2:\n" \
  "s_waitcnt lgkmcnt(10)\n" \
  "v_mfma_f32_16x16x32_bf16 v[128:131], v[80:83], v[96:99], v[128:131]\n" \
  "s_waitcnt lgkmcnt(9)\n" \
  "v_mfma_f32_16x16x32_bf16 v[132:135], v[84:87], v[96:99], v[132:135]\n" \
  "s_waitcnt lgkmcnt(8)\n" \
  "v_mfma_f32_16x16x32_bf16 v[136:139], v[88:91], v[96:99], v[136:139]\n" \
  "s_waitcnt lgkmcnt(7)\n" \
  "v_mfma_f32_16x16x32_bf16 v[140:143], v[92:95], v[96:99], v[140:143]\n" \
  "s_waitcnt lgkmcnt(6)\n" \
  "v_mfma_f32_16x16x32_bf16 v[156:159], v[92:95], v[100:103], v[156:159]\n" \
  "v_mfma_f32_16x16x32_bf16 v[152:155], v[88:91], v[100:103], v[152:155]\n" \
  "v_mfma_f32_16x16x32_bf16 v[148:151], v[84:87], v[100:103], v[148:151]\n" \
  "v_mfma_f32_16x16x32_bf16 v[144:147], v[80:83], v[100:103], v[144:147]\n" \
  "s_waitcnt lgkmcnt(0)\n" \
  "s_barrier\n" \
  "v_mfma_f32_16x16x32_bf16 v[160:163], v[80:83], v[104:107], v[160:163]\n" \
  "ds_read_b128 v[32:35], %[a0] offset:0\n" \
  "v_mfma_f32_16x16x32_bf16 v[164:167], v[84:87], v[104:107], v[164:167]\n" \
  "ds_read_b128 v[48:51], %[b0] offset:0\n" \
  "v_mfma_f32_16x16x32_bf16 v[168:171], v[88:91], v[104:107], v[168:171]\n" \
  "ds_read_b128 v[36:39], %[a0] offset:2048\n" \
  "v_mfma_f32_16x16x32_bf16 v[172:175], v[92:95], v[104:107], v[172:175]\n" \
  "ds_read_b128 v[40:43], %[a0] offset:4096\n" \
  "v_mfma_f32_16x16x32_bf16 v[188:191], v[92:95], v[108:111], v[188:191]\n" \
  "ds_read_b128 v[44:47], %[a0] offset:6144\n" \
  "v_mfma_f32_16x16x32_bf16 v[184:187], v[88:91], v[108:111], v[184:187]\n" \
  "ds_read_b128 v[52:55], %[b0] offset:2080\n" \
  "v_mfma_f32_16x16x32_bf16 v[180:183], v[84:87], v[108:111], v[180:183]\n" \
  "ds_read_b128 v[56:59], %[b0] offset:4160\n" \
  "v_mfma_f32_16x16x32_bf16 v[176:179], v[80:83], v[108:111], v[176:179]\n" \
  "ds_read_b128 v[60:63], %[b0] offset:6240\n" \
  "v_mfma_f32_16x16x32_bf16 v[192:195], v[80:83], v[112:115], v[192:195]\n" \
  "ds_read_b128 v[64:67], %[b0] offset:8320\n" \
  "v_mfma_f32_16x16x32_bf16 v[196:199], v[84:87], v[112:115], v[196:199]\n" \
  "ds_read_b128 v[68:71], %[b0] offset:10400\n" \
  "v_mfma_f32_16x16x32_bf16 v[200:203], v[88:91], v[112:115], v[200:203]\n" \
  "ds_read_b128 v[72:75], %[b0] offset:12480\n" \
  "v_mfma_f32_16x16x32_bf16 v[204:207], v[92:95], v[112:115], v[204:207]\n" \
  "ds_read_b128 v[76:79], %[b0] offset:14560\n" \
  "v_mfma_f32_16x16x32_bf16 v[220:223], v[92:95], v[116:119], v[220:223]\n" \
  "v_mfma_f32_16x16x32_bf16 v[216:219], v[88:91], v[116:119], v[216:219]\n" \
  "v_mfma_f32_16x16x32_bf16 v[212:215], v[84:87], v[116:119], v[212:215]\n" \
  "v_mfma_f32_16x16x32_bf16 v[208:211], v[80:83], v[116:119], v[208:211]\n" \
  "v_mfma_f32_16x16x32_bf16 v[224:227], v[80:83], v[120:123], v[224:227]\n" \
  "v_mfma_f32_16x16x32_bf16 v[228:231], v[84:87], v[120:123], v[228:231]\n" \
  "v_mfma_f32_16x16x32_bf16 v[232:235], v[88:91], v[120:123], v[232:235]\n" \
  "v_mfma_f32_16x16x32_bf16 v[236:239], v[92:95], v[120:123], v[236:239]\n" \
  "v_mfma_f32_16x16x32_bf16 v[252:255], v[92:95], v[124:127], v[252:255]\n" \
  "s_sub_u32 %[cnt], %[cnt], 1\n" \
  "s_cmp_lg_u32 %[cnt], 0\n" \
  "v_mfma_f32_16x16x32_bf16 v[248:251], v[88:91], v[124:127], v[248:251]\n" \
  "v_mfma_f32_16x16x32_bf16 v[244:247], v[84:87], v[124:127], v[244:247]\n" \
  "v_mfma_f32_16x16x32_bf16 v[240:243], v[80:83], v[124:127], v[240:243]\n" \
  "s_cbranch_scc1 1b\n" \
  "s_waitcnt lgkmcnt(10)\n" \
  "v_mfma_f32_16x16x32_bf16 v[128:131], v[32:35], v[48:51], v[128:131]\n" \
  "ds_read_b128 v[80:83], %[tA] offset:1024\n" \
  "s_waitcnt lgkmcnt(10)\n" \
  "v_mfma_f32_16x16x32_bf16 v[132:135], v[36:39], v[48:51], v[132:135]\n" \
  "ds_read_b128 v[96:99], %[tB] offset:1024\n" \
  "s_waitcnt lgkmcnt(8)\n" \
  "v_mfma_f32_16x16x32_bf16 v[148:151], v[36:39], v[52:55], v[148:151]\n" \
  "ds_read_b128 v[84:87], %[tA] offset:3072\n" \
  "v_mfma_f32_16x16x32_bf16 v[144:147], v[32:35], v[52:55], v[144:147]\n" \
  "ds_read_b128 v[100:103], %[tB] offset:3104\n" \
  "s_waitcnt lgkmcnt(9)\n" \
  "v_mfma_f32_16x16x32_bf16 v[160:163], v[32:35], v[56:59], v[160:163]\n" \
  "ds_read_b128 v[104:107], %[tB] offset:5184\n" \
  "v_mfma_f32_16x16x32_bf16 v[164:167], v[36:39], v[56:59], v[164:167]\n" \
  "ds_read_b128 v[108:111], %[tB] offset:7264\n" \
  "s_waitcnt lgkmcnt(10)\n" \
  "v_mfma_f32_16x16x32_bf16 v[180:183], v[36:39], v[60:63], v[180:183]\n" \
  "ds_read_b128 v[112:115], %[tB] offset:9344\n" \
  "v_mfma_f32_16x16x32_bf16 v[176:179], v[32:35], v[60:63], v[176:179]\n" \
  "ds_read_b128 v[116:119], %[tB] offset:11424\n" \
  "s_waitcnt lgkmcnt(11)\n" \
  "v_mfma_f32_16x16x32_bf16 v[192:195], v[32:35], v[64:67], v[192:195]\n" \
  "ds_read_b128 v[120:123], %[tB] offset:13504\n" \
  "v_mfma_f32_16x16x32_bf16 v[196:199], v[36:39], v[64:67], v[196:199]\n" \
  "ds_read_b128 v[124:127], %[tB] offset:15584\n" \
  "s_waitcnt lgkmcnt(12)\n" \
  "v_mfma_f32_16x16x32_bf16 v[212:215], v[36:39], v[68:71], v[212:215]\n" \
  "v_mfma_f32_16x16x32_bf16 v[208:211], v[32:35], v[68:71], v[208:211]\n" \
  "s_waitcnt lgkmcnt(11)\n" \
  "v_mfma_f32_16x16x32_bf16 v[224:227], v[32:35], v[72:75], v[224:227]\n" \
  "v_mfma_f32_16x16x32_bf16 v[228:231], v[36:39], v[72:75], v[228:231]\n" \
  "s_waitcnt lgkmcnt(10)\n" \
  "v_mfma_f32_16x16x32_bf16 v[244:247], v[36:39], v[76:79], v[244:247]\n" \
  "v_mfma_f32_16x16x32_bf16 v[240:243], v[32:35], v[76:79], v[240:243]\n" \
  "s_waitcnt lgkmcnt(8)\n" \
  "v_mfma_f32_16x16x32_bf16 v[128:131], v[80:83], v[96:99], v[128:131]\n" \
  "s_waitcnt lgkmcnt(7)\n" \
  "v_mfma_f32_16x16x32_bf16 v[132:135], v[84:87], v[96:99], v[132:135]\n" \
  "s_waitcnt lgkmcnt(6)\n" \
  "v_mfma_f32_16x16x32_bf16 v[148:151], v[84:87], v[100:103], v[148:151]\n" \
  "v_mfma_f32_16x16x32_bf16 v[144:147], v[80:83], v[100:103], v[144:147]\n" \
  "s_waitcnt lgkmcnt(5)\n" \
  "v_mfma_f32_16x16x32_bf16 v[160:163], v[80:83], v[104:107], v[160:163]\n" \
  "s_barrier\n" \
  "v_mfma_f32_16x16x32_bf16 v[164:167], v[84:87], v[104:107], v[164:167]\n" \
  "ds_read_b128 v[32:35], %[tA] offset:32768\n" \
  "s_waitcnt lgkmcnt(5)\n" \
  "v_mfma_f32_16x16x32_bf16 v[180:183], v[84:87], v[108:111], v[180:183]\n" \
  "ds_read_b128 v[48:51], %[tB] offset:16640\n" \
  "v_mfma_f32_16x16x32_bf16 v[176:179], v[80:83], v[108:111], v[176:179]\n" \
  "ds_read_b128 v[36:39], %[tA] offset:34816\n" \
  "s_waitcnt lgkmcnt(6)\n" \
  "v_mfma_f32_16x16x32_bf16 v[192:195], v[80:83], v[112:115], v[192:195]\n" \
  "ds_read_b128 v[52:55], %[tB] offset:18720\n" \
  "v_mfma_f32_16x16x32_bf16 v[196:199], v[84:87], v[112:115], v[196:199]\n" \
  "ds_read_b128 v[56:59], %[tB] offset:20800\n" \
  "s_waitcnt lgkmcnt(7)\n" \
  "v_mfma_f32_16x16x32_bf16 v[212:215], v[84:87], v[116:119], v[212:215]\n" \
  "ds_read_b128 v[60:63], %[tB] offset:22880\n" \
  "v_mfma_f32_16x16x32_bf16 v[208:211], v[80:83], v[116:119], v[208:211]\n" \
  "ds_read_b128 v[64:67], %[tB] offset:24960\n" \
  "s_waitcnt lgkmcnt(8)\n" \
  "v_mfma_f32_16x16x32_bf16 v[224:227], v[80:83], v[120:123], v[224:227]\n" \
  "ds_read_b128 v[68:71], %[tB] offset:27040\n" \
  "v_mfma_f32_16x16x32_bf16 v[228:231], v[84:87], v[120:123], v[228:231]\n" \
  "ds_read_b128 v[72:75], %[tB] offset:29120\n" \
  "s_waitcnt lgkmcnt(9)\n" \
  "v_mfma_f32_16x16x32_bf16 v[244:247], v[84:87], v[124:127], v[244:247]\n" \
  "ds_read_b128 v[76:79], %[tB] offset:31200\n" \
  "v_mfma_f32_16x16x32_bf16 v[240:243], v[80:83], v[124:127], v[240:243]\n" \
  "s_waitcnt lgkmcnt(8)\n" \
  "v_mfma_f32_16x16x32_bf16 v[128:131], v[32:35], v[48:51], v[128:131]\n" \
  "ds_read_b128 v[80:83], %[tA] offset:33792\n" \
  "s_waitcnt lgkmcnt(8)\n" \
  "v_mfma_f32_16x16x32_bf16 v[132:135], v[36:39], v[48:51], v[132:135]\n" \
  "ds_read_b128 v[96:99], %[tB] offset:17664\n" \
  "s_waitcnt lgkmcnt(8)\n" \
  "v_mfma_f32_16x16x32_bf16 v[148:151], v[36:39], v[52:55], v[148:151]\n" \
  "ds_read_b128 v[84:87], %[tA] offset:35840\n" \
  "v_mfma_f32_16x16x32_bf16 v[144:147], v[32:35], v[52:55], v[144:147]\n" \
  "ds_read_b128 v[100:103], %[tB] offset:19744\n" \
  "s_waitcnt lgkmcnt(9)\n" \
  "v_mfma_f32_16x16x32_bf16 v[160:163], v[32:35], v[56:59], v[160:163]\n" \
  "ds_read_b128 v[104:107], %[tB] offset:21824\n" \
  "v_mfma_f32_16x16x32_bf16 v[164:167], v[36:39], v[56:59], v[164:167]\n" \
  "ds_read_b128 v[108:111], %[tB] offset:23904\n" \
  "s_waitcnt lgkmcnt(10)\n" \
  "v_mfma_f32_16x16x32_bf16 v[180:183], v[36:39], v[60:63], v[180:183]\n" \
  "ds_read_b128 v[112:115], %[tB] offset:25984\n" \
  "v_mfma_f32_16x16x32_bf16 v[176:179], v[32:35], v[60:63], v[176:179]\n" \
  "ds_read_b128 v[116:119], %[tB] offset:28064\n" \
  "s_waitcnt lgkmcnt(11)\n" \
  "v_mfma_f32_16x16x32_bf16 v[192:195], v[32:35], v[64:67], v[192:195]\n" \
  "ds_read_b128 v[120:123], %[tB] offset:30144\n" \
  "v_mfma_f32_16x16x32_bf16 v[196:199], v[36:39], v[64:67], v[196:199]\n" \
  "ds_read_b128 v[124:127], %[tB] offset:32224\n" \
  "s_waitcnt lgkmcnt(12)\n" \
  "v_mfma_f32_16x16x32_bf16 v[212:215], v[36:39], v[68:71], v[212:215]\n" \
  "v_mfma_f32_16x16x32_bf16 v[208:211], v[32:35], v[68:71], v[208:211]\n" \
  "s_waitcnt lgkmcnt(11)\n" \
  "v_mfma_f32_16x16x32_bf16 v[224:227], v[32:35], v[72:75], v[224:227]\n" \
  "v_mfma_f32_16x16x32_bf16 v[228:231], v[36:39], v[72:75], v[228:231]\n" \
  "s_waitcnt lgkmcnt(10)\n" \
  "v_mfma_f32_16x16x32_bf16 v[244:247], v[36:39], v[76:79], v[244:247]\n" \
  "v_mfma_f32_16x16x32_bf16 v[240:243], v[32:35], v[76:79], v[240:243]\n" \
  "s_waitcnt lgkmcnt(8)\n" \
  "v_mfma_f32_16x16x32_bf16 v[128:131], v[80:83], v[96:99], v[128:131]\n" \
  "s_waitcnt lgkmcnt(7)\n" \
  "v_mfma_f32_16x16x32_bf16 v[132:135], v[84:87], v[96:99], v[132:135]\n" \
  "s_waitcnt lgkmcnt(6)\n" \
  "v_mfma_f32_16x16x32_bf16 v[148:151], v[84:87], v[100:103], v[148:151]\n" \
  "v_mfma_f32_16x16x32_bf16 v[144:147], v[80:83], v[100:103], v[144:147]\n" \
  "s_waitcnt lgkmcnt(5)\n" \
  "v_mfma_f32_16x16x32_bf16 v[160:163], v[80:83], v[104:107], v[160:163]\n" \
  "s_barrier\n" \
  "v_mfma_f32_16x16x32_bf16 v[164:167], v[84:87], v[104:107], v[164:167]\n" \
  "ds_read_b128 v[32:35], %[tH] offset:0\n" \
  "s_waitcnt lgkmcnt(5)\n" \
  "v_mfma_f32_16x16x32_bf16 v[180:183], v[84:87], v[108:111], v[180:183]\n" \
  "ds_read_b128 v[48:51], %[tB] offset:33280\n" \
  "v_mfma_f32_16x16x32_bf16 v[176:179], v[80:83], v[108:111], v[176:179]\n" \
  "ds_read_b128 v[36:39], %[tH] offset:2048\n" \
  "s_waitcnt lgkmcnt(6)\n" \
  "v_mfma_f32_16x16x32_bf16 v[192:195], v[80:83], v[112:115], v[192:195]\n" \
  "ds_read_b128 v[52:55], %[tB] offset:35360\n" \
  "v_mfma_f32_16x16x32_bf16 v[196:199], v[84:87], v[112:115], v[196:199]\n" \
  "ds_read_b128 v[56:59], %[tB] offset:37440\n" \
  "s_waitcnt lgkmcnt(7)\n" \
  "v_mfma_f32_16x16x32_bf16 v[212:215], v[84:87], v[116:119], v[212:215]\n" \
  "ds_read_b128 v[60:63], %[tB] offset:39520\n" \
  "v_mfma_f32_16x16x32_bf16 v[208:211], v[80:83], v[116:119], v[208:211]\n" \
  "ds_read_b128 v[64:67], %[tB] offset:41600\n" \
  "s_waitcnt lgkmcnt(8)\n" \
  "v_mfma_f32_16x16x32_bf16 v[224:227], v[80:83], v[120:123], v[224:227]\n" \
  "ds_read_b128 v[68:71], %[tB] offset:43680\n" \
  "v_mfma_f32_16x16x32_bf16 v[228:231], v[84:87], v[120:123], v[228:231]\n" \
  "ds_read_b128 v[72:75], %[tB] offset:45760\n" \
  "s_waitcnt lgkmcnt(9)\n" \
  "v_mfma_f32_16x16x32_bf16 v[244:247], v[84:87], v[124:127], v[244:247]\n" \
  "ds_read_b128 v[76:79], %[tB] offset:47840\n" \
  "v_mfma_f32_16x16x32_bf16 v[240:243], v[80:83], v[124:127], v[240:243]\n" \
  "s_waitcnt lgkmcnt(8)\n" \
  "v_mfma_f32_16x16x32_bf16 v[128:131], v[32:35], v[48:51], v[128:131]\n" \
  "ds_read_b128 v[80:83], %[tH] offset:1024\n" \
  "s_waitcnt lgkmcnt(8)\n" \
  "v_mfma_f32_16x16x32_bf16 v[132:135], v[36:39], v[48:51], v[132:135]\n" \
  "ds_read_b128 v[96:99], %[tB] offset:34304\n" \
  "s_waitcnt lgkmcnt(8)\n" \
  "v_mfma_f32_16x16x32_bf16 v[148:151], v[36:39], v[52:55], v[148:151]\n" \
  "ds_read_b128 v[84:87], %[tH] offset:3072\n" \
  "v_mfma_f32_16x16x32_bf16 v[144:147], v[32:35], v[52:55], v[144:147]\n" \
  "ds_read_b128 v[100:103], %[tB] offset:36384\n" \
  "s_waitcnt lgkmcnt(9)\n" \
  "v_mfma_f32_16x16x32_bf16 v[160:163], v[32:35], v[56:59], v[160:163]\n" \
  "ds_read_b128 v[104:107], %[tB] offset:38464\n" \
  "v_mfma_f32_16x16x32_bf16 v[164:167], v[36:39], v[56:59], v[164:167]\n" \
  "ds_read_b128 v[108:111], %[tB] offset:40544\n" \
  "s_waitcnt lgkmcnt(10)\n" \
  "v_mfma_f32_16x16x32_bf16 v[180:183], v[36:39], v[60:63], v[180:183]\n" \
  "ds_read_b128 v[112:115], %[tB] offset:42624\n" \
  "v_mfma_f32_16x16x32_bf16 v[176:179], v[32:35], v[60:63], v[176:179]\n" \
  "ds_read_b128 v[116:119], %[tB] offset:44704\n" \
  "s_waitcnt lgkmcnt(11)\n" \
  "v_mfma_f32_16x16x32_bf16 v[192:195], v[32:35], v[64:67], v[192:195]\n" \
  "ds_read_b128 v[120:123], %[tB] offset:46784\n" \
  "v_mfma_f32_16x16x32_bf16 v[196:199], v[36:39], v[64:67], v[196:199]\n" \
  "ds_read_b128 v[124:127], %[tB] offset:48864\n" \
  "s_waitcnt lgkmcnt(12)\n" \
  "v_mfma_f32_16x16x32_bf16 v[212:215], v[36:39], v[68:71], v[212:215]\n" \
  "v_mfma_f32_16x16x32_bf16 v[208:211], v[32:35], v[68:71], v[208:211]\n" \
  "s_waitcnt lgkmcnt(11)\n" \
  "v_mfma_f32_16x16x32_bf16 v[224:227], v[32:35], v[72:75], v[224:227]\n" \
  "v_mfma_f32_16x16x32_bf16 v[228:231], v[36:39], v[72:75], v[228:231]\n" \
  "s_waitcnt lgkmcnt(10)\n" \
  "v_mfma_f32_16x16x32_bf16 v[244:247], v[36:39], v[76:79], v[244:247]\n" \
  "v_mfma_f32_16x16x32_bf16 v[240:243], v[32:35], v[76:79], v[240:243]\n" \
  "s_waitcnt lgkmcnt(8)\n" \
  "v_mfma_f32_16x16x32_bf16 v[128:131], v[80:83], v[96:99], v[128:131]\n" \
  "ds_read_b128 v[32:35], %[tA] offset:4096\n" \
  "s_waitcnt lgkmcnt(8)\n" \
  "v_mfma_f32_16x16x32_bf16 v[132:135], v[84:87], v[96:99], v[132:135]\n" \
  "ds_read_b128 v[48:51], %[tB] offset:0\n" \
  "s_waitcnt lgkmcnt(8)\n" \
  "v_mfma_f32_16x16x32_bf16 v[148:151], v[84:87], v[100:103], v[148:151]\n" \
  "ds_read_b128 v[36:39], %[tA] offset:6144\n" \
  "v_mfma_f32_16x16x32_bf16 v[144:147], v[80:83], v[100:103], v[144:147]\n" \
  "ds_read_b128 v[52:55], %[tB] offset:2080\n" \
  "s_waitcnt lgkmcnt(9)\n" \
  "v_mfma_f32_16x16x32_bf16 v[160:163], v[80:83], v[104:107], v[160:163]\n" \
  "ds_read_b128 v[56:59], %[tB] offset:4160\n" \
  "v_mfma_f32_16x16x32_bf16 v[164:167], v[84:87], v[104:107], v[164:167]\n" \
  "ds_read_b128 v[60:63], %[tB] offset:6240\n" \
  "s_waitcnt lgkmcnt(10)\n" \
  "v_mfma_f32_16x16x32_bf16 v[180:183], v[84:87], v[108:111], v[180:183]\n" \
  "ds_read_b128 v[64:67], %[tB] offset:8320\n" \
  "v_mfma_f32_16x16x32_bf16 v[176:179], v[80:83], v[108:111], v[176:179]\n" \
  "ds_read_b128 v[68:71], %[tB] offset:10400\n" \
  "s_waitcnt lgkmcnt(11)\n" \
  "v_mfma_f32_16x16x32_bf16 v[192:195], v[80:83], v[112:115], v[192:195]\n" \
  "ds_read_b128 v[72:75], %[tB] offset:12480\n" \
  "v_mfma_f32_16x16x32_bf16 v[196:199], v[84:87], v[112:115], v[196:199]\n" \
  "ds_read_b128 v[76:79], %[tB] offset:14560\n" \
  "s_waitcnt lgkmcnt(12)\n" \
  "v_mfma_f32_16x16x32_bf16 v[212:215], v[84:87], v[116:119], v[212:215]\n" \
  "v_mfma_f32_16x16x32_bf16 v[208:211], v[80:83], v[116:119], v[208:211]\n" \
  "s_waitcnt lgkmcnt(11)\n" \
  "v_mfma_f32_16x16x32_bf16 v[224:227], v[80:83], v[120:123], v[224:227]\n" \
  "v_mfma_f32_16x16x32_bf16 v[228:231], v[84:87], v[120:123], v[228:231]\n" \
  "s_waitcnt lgkmcnt(10)\n" \
  "v_mfma_f32_16x16x32_bf16 v[244:247], v[84:87], v[124:127], v[244:247]\n" \
  "v_mfma_f32_16x16x32_bf16 v[240:243], v[80:83], v[124:127], v[240:243]\n" \
  "s_waitcnt lgkmcnt(8)\n" \
  "v_mfma_f32_16x16x32_bf16 v[136:139], v[32:35], v[48:51], v[136:139]\n" \
  "ds_read_b128 v[80:83], %[tA] offset:5120\n" \
  "s_waitcnt lgkmcnt(8)\n" \
  "v_mfma_f32_16x16x32_bf16 v[140:143], v[36:39], v[48:51], v[140:143]\n" \
  "ds_read_b128 v[96:99], %[tB] offset:1024\n" \
  "s_waitcnt lgkmcnt(8)\n" \
  "v_mfma_f32_16x16x32_bf16 v[156:159], v[36:39], v[52:55], v[156:159]\n" \
  "ds_read_b128 v[84:87], %[tA] offset:7168\n" \
  "v_mfma_f32_16x16x32_bf16 v[152:155], v[32:35], v[52:55], v[152:155]\n" \
  "ds_read_b128 v[100:103], %[tB] offset:3104\n" \
  "s_waitcnt lgkmcnt(9)\n" \
  "v_mfma_f32_16x16x32_bf16 v[168:171], v[32:35], v[56:59], v[168:171]\n" \
  "ds_read_b128 v[104:107], %[tB] offset:5184\n" \
  "v_cvt_pk_bf16_f32 v40, v128, v144\n" \
  "v_cvt_pk_bf16_f32 v41, v160, v176\n" \
  "v_mfma_f32_16x16x32_bf16 v[172:175], v[36:39], v[56:59], v[172:175]\n" \
  "ds_read_b128 v[108:111], %[tB] offset:7264\n" \
  "v_cvt_pk_bf16_f32 v42, v192, v208\n" \
  "v_cvt_pk_bf16_f32 v43, v224, v240\n" \
  "s_waitcnt lgkmcnt(10)\n" \
  "v_mfma_f32_16x16x32_bf16 v[188:191], v[36:39], v[60:63], v[188:191]\n" \
  "ds_read_b128 v[112:115], %[tB] offset:9344\n" \
  "s_mov_b32 %[dA], 0\n" \
  "buffer_store_dwordx4 v[40:43], %[oy], %[ry], %[dA] offen" YSC "\n" \
  "v_mfma_f32_16x16x32_bf16 v[184:187], v[32:35], v[60:63], v[184:187]\n" \
  "ds_read_b128 v[116:119], %[tB] offset:11424\n" \
  "s_waitcnt lgkmcnt(11)\n" \
  "v_mfma_f32_16x16x32_bf16 v[200:203], v[32:35], v[64:67], v[200:203]\n" \
  "ds_read_b128 v[120:123], %[tB] offset:13504\n" \
  "v_mfma_f32_16x16x32_bf16 v[204:207], v[36:39], v[64:67], v[204:207]\n" \
  "ds_read_b128 v[124:127], %[tB] offset:15584\n" \
  "s_waitcnt lgkmcnt(12)\n" \
  "v_mfma_f32_16x16x32_bf16 v[220:223], v[36:39], v[68:71], v[220:223]\n" \
  "v_mfma_f32_16x16x32_bf16 v[216:219], v[32:35], v[68:71], v[216:219]\n" \
  "s_waitcnt lgkmcnt(11)\n" \
  "v_mfma_f32_16x16x32_bf16 v[232:235], v[32:35], v[72:75], v[232:235]\n" \
  "v_mfma_f32_16x16x32_bf16 v[236:239], v[36:39], v[72:75], v[236:239]\n" \
  "s_waitcnt lgkmcnt(10)\n" \
  "v_mfma_f32_16x16x32_bf16 v[252:255], v[36:39], v[76:79], v[252:255]\n" \
  "v_cvt_pk_bf16_f32 v44, v129, v145\n" \
  "v_cvt_pk_bf16_f32 v45, v161, v177\n" \
  "v_mfma_f32_16x16x32_bf16 v[248:251], v[32:35], v[76:79], v[248:251]\n" \
  "v_cvt_pk_bf16_f32 v46, v193, v209\n" \
  "v_cvt_pk_bf16_f32 v47, v225, v241\n" \
  "s_waitcnt lgkmcnt(8)\n" \
  "v_mfma_f32_16x16x32_bf16 v[136:139], v[80:83], v[96:99], v[136:139]\n" \
  "ds_read_b128 v[32:35], %[tA] offset:36864\n" \
  "s_mov_b32 %[dA], 7168\n" \
  "buffer_store_dwordx4 v[44:47], %[oy], %[ry], %[dA] offen" YSC "\n" \
  "s_waitcnt lgkmcnt(8)\n" \
  "v_mfma_f32_16x16x32_bf16 v[140:143], v[84:87], v[96:99], v[140:143]\n" \
  "ds_read_b128 v[48:51], %[tB] offset:16640\n" \
  "s_waitcnt lgkmcnt(8)\n" \
  "v_mfma_f32_16x16x32_bf16 v[156:159], v[84:87], v[100:103], v[156:159]\n" \
  "ds_read_b128 v[36:39], %[tA] offset:38912\n" \
  "v_mfma_f32_16x16x32_bf16 v[152:155], v[80:83], v[100:103], v[152:155]\n" \
  "ds_read_b128 v[52:55], %[tB] offset:18720\n" \
  "s_waitcnt lgkmcnt(9)\n" \
  "v_mfma_f32_16x16x32_bf16 v[168:171], v[80:83], v[104:107], v[168:171]\n" \
  "ds_read_b128 v[56:59], %[tB] offset:20800\n" \
  "v_mfma_f32_16x16x32_bf16 v[172:175], v[84:87], v[104:107], v[172:175]\n" \
  "ds_read_b128 v[60:63], %[tB] offset:22880\n" \
  "s_waitcnt lgkmcnt(10)\n" \
  "v_mfma_f32_16x16x32_bf16 v[188:191], v[84:87], v[108:111], v[188:191]\n" \
  "ds_read_b128 v[64:67], %[tB] offset:24960\n" \
  "v_mfma_f32_16x16x32_bf16 v[184:187], v[80:83], v[108:111], v[184:187]\n" \
  "ds_read_b128 v[68:71], %[tB] offset:27040\n" \
  "s_waitcnt lgkmcnt(11)\n" \
  "v_mfma_f32_16x16x32_bf16 v[200:203], v[80:83], v[112:115], v[200:203]\n" \
  "ds_read_b128 v[72:75], %[tB] offset:29120\n" \
  "v_cvt_pk_bf16_f32 v88, v130, v146\n" \
  "v_cvt_pk_bf16_f32 v89, v162, v178\n" \
  "v_mfma_f32_16x16x32_bf16 v[204:207], v[84:87], v[112:115], v[204:207]\n" \
  "ds_read_b128 v[76:79], %[tB] offset:31200\n" \
  "v_cvt_pk_bf16_f32 v90, v194, v210\n" \
  "v_cvt_pk_bf16_f32 v91, v226, v242\n" \
  "s_waitcnt lgkmcnt(12)\n" \
  "v_mfma_f32_16x16x32_bf16 v[220:223], v[84:87], v[116:119], v[220:223]\n" \
  "s_mov_b32 %[dA], 14336\n" \
  "buffer_store_dwordx4 v[88:91], %[oy], %[ry], %[dA] offen" YSC "\n" \
  "v_mfma_f32_16x16x32_bf16 v[216:219], v[80:83], v[116:119], v[216:219]\n" \
  "s_waitcnt lgkmcnt(11)\n" \
  "v_mfma_f32_16x16x32_bf16 v[232:235], v[80:83], v[120:123], v[232:235]\n" \
  "v_mfma_f32_16x16x32_bf16 v[236:239], v[84:87], v[120:123], v[236:239]\n" \
  "s_waitcnt lgkmcnt(10)\n" \
  "v_mfma_f32_16x16x32_bf16 v[252:255], v[84:87], v[124:127], v[252:255]\n" \
  "v_mfma_f32_16x16x32_bf16 v[248:251], v[80:83], v[124:127], v[248:251]\n" \
  "s_waitcnt lgkmcnt(8)\n" \
  "v_mfma_f32_16x16x32_bf16 v[136:139], v[32:35], v[48:51], v[136:139]\n" \
  "ds_read_b128 v[80:83], %[tA] offset:37888\n" \
  "s_waitcnt lgkmcnt(8)\n" \
  "v_mfma_f32_16x16x32_bf16 v[140:143], v[36:39], v[48:51], v[140:143]\n" \
  "ds_read_b128 v[96:99], %[tB] offset:17664\n" \
  "s_waitcnt lgkmcnt(8)\n" \
  "v_mfma_f32_16x16x32_bf16 v[156:159], v[36:39], v[52:55], v[156:159]\n" \
  "ds_read_b128 v[84:87], %[tA] offset:39936\n" \
  "v_cvt_pk_bf16_f32 v92, v131, v147\n" \
  "v_cvt_pk_bf16_f32 v93, v163, v179\n" \
  "v_mfma_f32_16x16x32_bf16 v[152:155], v[32:35], v[52:55], v[152:155]\n" \
  "ds_read_b128 v[100:103], %[tB] offset:19744\n" \
  "v_cvt_pk_bf16_f32 v94, v195, v211\n" \
  "v_cvt_pk_bf16_f32 v95, v227, v243\n" \
  "s_waitcnt lgkmcnt(9)\n" \
  "v_mfma_f32_16x16x32_bf16 v[168:171], v[32:35], v[56:59], v[168:171]\n" \
  "ds_read_b128 v[104:107], %[tB] offset:21824\n" \
  "s_mov_b32 %[dA], 21504\n" \
  "buffer_store_dwordx4 v[92:95], %[oy], %[ry], %[dA] offen" YSC "\n" \
  "v_mfma_f32_16x16x32_bf16 v[172:175], v[36:39], v[56:59], v[172:175]\n" \
  "ds_read_b128 v[108:111], %[tB] offset:23904\n" \
  "s_waitcnt lgkmcnt(10)\n" \
  "v_mfma_f32_16x16x32_bf16 v[188:191], v[36:39], v[60:63], v[188:191]\n" \
  "ds_read_b128 v[112:115], %[tB] offset:25984\n" \
  "v_mfma_f32_16x16x32_bf16 v[184:187], v[32:35], v[60:63], v[184:187]\n" \
  "ds_read_b128 v[116:119], %[tB] offset:28064\n" \
  "s_waitcnt lgkmcnt(11)\n" \
  "v_mfma_f32_16x16x32_bf16 v[200:203], v[32:35], v[64:67], v[200:203]\n" \
  "ds_read_b128 v[120:123], %[tB] offset:30144\n" \
  "v_mfma_f32_16x16x32_bf16 v[204:207], v[36:39], v[64:67], v[204:207]\n" \
  "ds_read_b128 v[124:127], %[tB] offset:32224\n" \
  "s_waitcnt lgkmcnt(12)\n" \
  "v_mfma_f32_16x16x32_bf16 v[220:223], v[36:39], v[68:71], v[220:223]\n" \
  "v_mfma_f32_16x16x32_bf16 v[216:219], v[32:35], v[68:71], v[216:219]\n" \
  "s_waitcnt lgkmcnt(11)\n" \
  "v_mfma_f32_16x16x32_bf16 v[232:235], v[32:35], v[72:75], v[232:235]\n" \
  "v_cvt_pk_bf16_f32 v40, v132, v148\n" \
  "v_cvt_pk_bf16_f32 v41, v164, v180\n" \
  "v_mfma_f32_16x16x32_bf16 v[236:239], v[36:39], v[72:75], v[236:239]\n" \
  "v_cvt_pk_bf16_f32 v42, v196, v212\n" \
  "v_cvt_pk_bf16_f32 v43, v228, v244\n" \
  "s_waitcnt lgkmcnt(10)\n" \
  "v_mfma_f32_16x16x32_bf16 v[252:255], v[36:39], v[76:79], v[252:255]\n" \
  "s_mov_b32 %[dA], 114688\n" \
  "buffer_store_dwordx4 v[40:43], %[oy], %[ry], %[dA] offen" YSC "\n" \
  "v_mfma_f32_16x16x32_bf16 v[248:251], v[32:35], v[76:79], v[248:251]\n" \
  "s_waitcnt lgkmcnt(8)\n" \
  "v_mfma_f32_16x16x32_bf16 v[136:139], v[80:83], v[96:99], v[136:139]\n" \
  "ds_read_b128 v[32:35], %[tH] offset:4096\n" \
  "s_waitcnt lgkmcnt(8)\n" \
  "v_mfma_f32_16x16x32_bf16 v[140:143], v[84:87], v[96:99], v[140:143]\n" \
  "ds_read_b128 v[48:51], %[tB] offset:33280\n" \
  "s_waitcnt lgkmcnt(8)\n" \
  "v_mfma_f32_16x16x32_bf16 v[156:159], v[84:87], v[100:103], v[156:159]\n" \
  "ds_read_b128 v[36:39], %[tH] offset:6144\n" \
  "v_mfma_f32_16x16x32_bf16 v[152:155], v[80:83], v[100:103], v[152:155]\n" \
  "ds_read_b128 v[52:55], %[tB] offset:35360\n" \
  "s_waitcnt lgkmcnt(9)\n" \
  "v_mfma_f32_16x16x32_bf16 v[168:171], v[80:83], v[104:107], v[168:171]\n" \
  "ds_read_b128 v[56:59], %[tB] offset:37440\n" \
  "v_mfma_f32_16x16x32_bf16 v[172:175], v[84:87], v[104:107], v[172:175]\n" \
  "ds_read_b128 v[60:63], %[tB] offset:39520\n" \
  "s_waitcnt lgkmcnt(10)\n" \
  "v_mfma_f32_16x16x32_bf16 v[188:191], v[84:87], v[108:111], v[188:191]\n" \
  "ds_read_b128 v[64:67], %[tB] offset:41600\n" \
  "v_cvt_pk_bf16_f32 v44, v133, v149\n" \
  "v_cvt_pk_bf16_f32 v45, v165, v181\n" \
  "v_mfma_f32_16x16x32_bf16 v[184:187], v[80:83], v[108:111], v[184:187]\n" \
  "ds_read_b128 v[68:71], %[tB] offset:43680\n" \
  "v_cvt_pk_bf16_f32 v46, v197, v213\n" \
  "v_cvt_pk_bf16_f32 v47, v229, v245\n" \
  "s_waitcnt lgkmcnt(11)\n" \
  "v_mfma_f32_16x16x32_bf16 v[200:203], v[80:83], v[112:115], v[200:203]\n" \
  "ds_read_b128 v[72:75], %[tB] offset:45760\n" \
  "s_mov_b32 %[dA], 121856\n" \
  "buffer_store_dwordx4 v[44:47], %[oy], %[ry], %[dA] offen" YSC "\n" \
  "v_mfma_f32_16x16x32_bf16 v[204:207], v[84:87], v[112:115], v[204:207]\n" \
  "ds_read_b128 v[76:79], %[tB] offset:47840\n" \
  "s_waitcnt lgkmcnt(12)\n" \
  "v_mfma_f32_16x16x32_bf16 v[220:223], v[84:87], v[116:119], v[220:223]\n" \
  "v_mfma_f32_16x16x32_bf16 v[216:219], v[80:83], v[116:119], v[216:219]\n" \
  "s_waitcnt lgkmcnt(11)\n" \
  "v_mfma_f32_16x16x32_bf16 v[232:235], v[80:83], v[120:123], v[232:235]\n" \
  "v_mfma_f32_16x16x32_bf16 v[236:239], v[84:87], v[120:123], v[236:239]\n" \
  "s_waitcnt lgkmcnt(10)\n" \
  "v_mfma_f32_16x16x32_bf16 v[252:255], v[84:87], v[124:127], v[252:255]\n" \
  "v_mfma_f32_16x16x32_bf16 v[248:251], v[80:83], v[124:127], v[248:251]\n" \
  "s_waitcnt lgkmcnt(8)\n" \
  "v_mfma_f32_16x16x32_bf16 v[136:139], v[32:35], v[48:51], v[136:139]\n" \
  "ds_read_b128 v[80:83], %[tH] offset:5120\n" \
  "v_cvt_pk_bf16_f32 v88, v134, v150\n" \
  "v_cvt_pk_bf16_f32 v89, v166, v182\n" \
  "s_waitcnt lgkmcnt(8)\n" \
  "v_mfma_f32_16x16x32_bf16 v[140:143], v[36:39], v[48:51], v[140:143]\n" \
  "ds_read_b128 v[96:99], %[tB] offset:34304\n" \
  "v_cvt_pk_bf16_f32 v90, v198, v214\n" \
  "v_cvt_pk_bf16_f32 v91, v230, v246\n" \
  "s_waitcnt lgkmcnt(8)\n" \
  "v_mfma_f32_16x16x32_bf16 v[156:159], v[36:39], v[52:55], v[156:159]\n" \
  "ds_read_b128 v[84:87], %[tH] offset:7168\n" \
  "s_mov_b32 %[dA], 129024\n" \
  "buffer_store_dwordx4 v[88:91], %[oy], %[ry], %[dA] offen" YSC "\n" \
  "v_mfma_f32_16x16x32_bf16 v[152:155], v[32:35], v[52:55], v[152:155]\n" \
  "ds_read_b128 v[100:103], %[tB] offset:36384\n" \
  "s_waitcnt lgkmcnt(9)\n" \
  "v_mfma_f32_16x16x32_bf16 v[168:171], v[32:35], v[56:59], v[168:171]\n" \
  "ds_read_b128 v[104:107], %[tB] offset:38464\n" \
  "v_mfma_f32_16x16x32_bf16 v[172:175], v[36:39], v[56:59], v[172:175]\n" \
  "ds_read_b128 v[108:111], %[tB] offset:40544\n" \
  "s_waitcnt lgkmcnt(10)\n" \
  "v_mfma_f32_16x16x32_bf16 v[188:191], v[36:39], v[60:63], v[188:191]\n" \
  "ds_read_b128 v[112:115], %[tB] offset:42624\n" \
  "v_mfma_f32_16x16x32_bf16 v[184:187], v[32:35], v[60:63], v[184:187]\n" \
  "ds_read_b128 v[116:119], %[tB] offset:44704\n" \
  "s_waitcnt lgkmcnt(11)\n" \
  "v_mfma_f32_16x16x32_bf16 v[200:203], v[32:35], v[64:67], v[200:203]\n" \
  "ds_read_b128 v[120:123], %[tB] offset:46784\n" \
  "v_mfma_f32_16x16x32_bf16 v[204:207], v[36:39], v[64:67], v[204:207]\n" \
  "ds_read_b128 v[124:127], %[tB] offset:48864\n" \
  "s_waitcnt lgkmcnt(12)\n" \
  "v_mfma_f32_16x16x32_bf16 v[220:223], v[36:39], v[68:71], v[220:223]\n" \
  "v_cvt_pk_bf16_f32 v92, v135, v151\n" \
  "v_cvt_pk_bf16_f32 v93, v167, v183\n" \
  "v_mfma_f32_16x16x32_bf16 v[216:219], v[32:35], v[68:71], v[216:219]\n" \
  "v_cvt_pk_bf16_f32 v94, v199, v215\n" \
  "v_cvt_pk_bf16_f32 v95, v231, v247\n" \
  "s_waitcnt lgkmcnt(11)\n" \
  "v_mfma_f32_16x16x32_bf16 v[232:235], v[32:35], v[72:75], v[232:235]\n" \
  "s_mov_b32 %[dA], 136192\n" \
  "buffer_store_dwordx4 v[92:95], %[oy], %[ry], %[dA] offen" YSC "\n" \
  "v_mfma_f32_16x16x32_bf16 v[236:239], v[36:39], v[72:75], v[236:239]\n" \
  "s_waitcnt lgkmcnt(10)\n" \
  "v_mfma_f32_16x16x32_bf16 v[252:255], v[36:39], v[76:79], v[252:255]\n" \
  "v_mfma_f32_16x16x32_bf16 v[248:251], v[32:35], v[76:79], v[248:251]\n" \
  "s_waitcnt lgkmcnt(8)\n" \
  "v_mfma_f32_16x16x32_bf16 v[136:139], v[80:83], v[96:99], v[136:139]\n" \
  "s_waitcnt lgkmcnt(7)\n" \
  "v_mfma_f32_16x16x32_bf16 v[140:143], v[84:87], v[96:99], v[140:143]\n" \
  "s_waitcnt lgkmcnt(6)\n" \
  "v_mfma_f32_16x16x32_bf16 v[156:159], v[84:87], v[100:103], v[156:159]\n" \
  "v_mfma_f32_16x16x32_bf16 v[152:155], v[80:83], v[100:103], v[152:155]\n" \
  "s_waitcnt lgkmcnt(5)\n" \
  "v_mfma_f32_16x16x32_bf16 v[168:171], v[80:83], v[104:107], v[168:171]\n" \
  "v_mfma_f32_16x16x32_bf16 v[172:175], v[84:87], v[104:107], v[172:175]\n" \
  "s_waitcnt lgkmcnt(4)\n" \
  "v_mfma_f32_16x16x32_bf16 v[188:191], v[84:87], v[108:111], v[188:191]\n" \
  "v_mfma_f32_16x16x32_bf16 v[184:187], v[80:83], v[108:111], v[184:187]\n" \
  "s_waitcnt lgkmcnt(3)\n" \
  "v_mfma_f32_16x16x32_bf16 v[200:203], v[80:83], v[112:115], v[200:203]\n" \
  "v_mfma_f32_16x16x32_bf16 v[204:207], v[84:87], v[112:115], v[204:207]\n" \
  "s_waitcnt lgkmcnt(2)\n" \
  "v_mfma_f32_16x16x32_bf16 v[220:223], v[84:87], v[116:119], v[220:223]\n" \
  "v_mfma_f32_16x16x32_bf16 v[216:219], v[80:83], v[116:119], v[216:219]\n" \
  "s_waitcnt lgkmcnt(1)\n" \
  "v_mfma_f32_16x16x32_bf16 v[232:235], v[80:83], v[120:123], v[232:235]\n" \
  "v_mfma_f32_16x16x32_bf16 v[236:239], v[84:87], v[120:123], v[236:239]\n" \
  "s_waitcnt lgkmcnt(0)\n" \
  "v_mfma_f32_16x16x32_bf16 v[252:255], v[84:87], v[124:127], v[252:255]\n" \
  "v_mfma_f32_16x16x32_bf16 v[248:251], v[80:83], v[124:127], v[248:251]\n" \
  "s_nop 7\n" \
  "s_nop 1\n"
#define DQ_UNP "v_lshlrev_b32 v64, 16, v116\n" "v_and_b32 v65, 0xffff0000, v116\n" "v_lshlrev_b32 v72, 16, v112\n" "v_and_b32 v73, 0xffff0000, v112\n" "v_lshlrev_b32 v66, 16, v117\n" "v_and_b32 v67, 0xffff0000, v117\n" "v_lshlrev_b32 v74, 16, v113\n" "v_and_b32 v75, 0xffff0000, v113\n" "v_lshlrev_b32 v68, 16, v118\n" "v_and_b32 v69, 0xffff0000, v118\n" "v_lshlrev_b32 v76, 16, v114\n" "v_and_b32 v77, 0xffff0000, v114\n" "v_lshlrev_b32 v70, 16, v119\n" "v_and_b32 v71, 0xffff0000, v119\n" "v_lshlrev_b32 v78, 16, v115\n" "v_and_b32 v79, 0xffff0000, v115\n"
#define DQ_SEQ0 "v_cvt_f32_i32 v80, v96\n" "v_cvt_f32_i32 v81, v97\n" "v_cvt_f32_i32 v82, v98\n" "v_cvt_f32_i32 v83, v99\n" "v_cvt_f32_i32 v84, v100\n" "v_cvt_f32_i32 v85, v101\n" "v_cvt_f32_i32 v86, v102\n" "v_cvt_f32_i32 v87, v103\n" "v_cvt_pk_bf16_f32 v80, 0, v80\n" "v_cvt_pk_bf16_f32 v81, 0, v81\n" "v_cvt_pk_bf16_f32 v82, 0, v82\n" "v_cvt_pk_bf16_f32 v83, 0, v83\n" "v_cvt_pk_bf16_f32 v84, 0, v84\n" "v_cvt_pk_bf16_f32 v85, 0, v85\n" "v_cvt_pk_bf16_f32 v86, 0, v86\n" "v_cvt_pk_bf16_f32 v87, 0, v87\n" "v_pk_add_f32 v[80:81], v[80:81], v[64:65] neg_lo:[0,1] neg_hi:[0,1]\n" "v_pk_add_f32 v[82:83], v[82:83], v[66:67] neg_lo:[0,1] neg_hi:[0,1]\n" "v_pk_add_f32 v[84:85], v[84:85], v[68:69] neg_lo:[0,1] neg_hi:[0,1]\n" "v_pk_add_f32 v[86:87], v[86:87], v[70:71] neg_lo:[0,1] neg_hi:[0,1]\n" "v_cvt_pk_bf16_f32 v80, 0, v80\n" "v_cvt_pk_bf16_f32 v81, 0, v81\n" "v_cvt_pk_bf16_f32 v82, 0, v82\n" "v_cvt_pk_bf16_f32 v83, 0, v83\n" "v_cvt_pk_bf16_f32 v84, 0, v84\n" "v_cvt_pk_bf16_f32 v85, 0, v85\n" "v_cvt_pk_bf16_f32 v86, 0, v86\n" "v_cvt_pk_bf16_f32 v87, 0, v87\n" "v_pk_mul_f32 v[80:81], v[80:81], v[72:73]\n" "v_pk_mul_f32 v[82:83], v[82:83], v[74:75]\n" "v_pk_mul_f32 v[84:85], v[84:85], v[76:77]\n" "v_pk_mul_f32 v[86:87], v[86:87], v[78:79]\n" "v_cvt_pk_bf16_f32 v88, v80, v81\n" "v_cvt_pk_bf16_f32 v89, v82, v83\n" "v_cvt_pk_bf16_f32 v90, v84, v85\n" "v_cvt_pk_bf16_f32 v91, v86, v87\n"
#define DQ_SEQ1 "v_cvt_f32_i32 v80, v104\n" "v_cvt_f32_i32 v81, v105\n" "v_cvt_f32_i32 v82, v106\n" "v_cvt_f32_i32 v83, v107\n" "v_cvt_f32_i32 v84, v108\n" "v_cvt_f32_i32 v85, v109\n" "v_cvt_f32_i32 v86, v110\n" "v_cvt_f32_i32 v87, v111\n" "v_cvt_pk_bf16_f32 v80, 0, v80\n" "v_cvt_pk_bf16_f32 v81, 0, v81\n" "v_cvt_pk_bf16_f32 v82, 0, v82\n" "v_cvt_pk_bf16_f32 v83, 0, v83\n" "v_cvt_pk_bf16_f32 v84, 0, v84\n" "v_cvt_pk_bf16_f32 v85, 0, v85\n" "v_cvt_pk_bf16_f32 v86, 0, v86\n" "v_cvt_pk_bf16_f32 v87, 0, v87\n" "v_pk_add_f32 v[80:81], v[80:81], v[64:65] neg_lo:[0,1] neg_hi:[0,1]\n" "v_pk_add_f32 v[82:83], v[82:83], v[66:67] neg_lo:[0,1] neg_hi:[0,1]\n" "v_pk_add_f32 v[84:85], v[84:85], v[68:69] neg_lo:[0,1] neg_hi:[0,1]\n" "v_pk_add_f32 v[86:87], v[86:87], v[70:71] neg_lo:[0,1] neg_hi:[0,1]\n" "v_cvt_pk_bf16_f32 v80, 0, v80\n" "v_cvt_pk_bf16_f32 v81, 0, v81\n" "v_cvt_pk_bf16_f32 v82, 0, v82\n" "v_cvt_pk_bf16_f32 v83, 0, v83\n" "v_cvt_pk_bf16_f32 v84, 0, v84\n" "v_cvt_pk_bf16_f32 v85, 0, v85\n" "v_cvt_pk_bf16_f32 v86, 0, v86\n" "v_cvt_pk_bf16_f32 v87, 0, v87\n" "v_pk_mul_f32 v[80:81], v[80:81], v[72:73]\n" "v_pk_mul_f32 v[82:83], v[82:83], v[74:75]\n" "v_pk_mul_f32 v[84:85], v[84:85], v[76:77]\n" "v_pk_mul_f32 v[86:87], v[86:87], v[78:79]\n" "v_cvt_pk_bf16_f32 v92, v80, v81\n" "v_cvt_pk_bf16_f32 v93, v82, v83\n" "v_cvt_pk_bf16_f32 v94, v84, v85\n" "v_cvt_pk_bf16_f32 v95, v86, v87\n"

static_assert(WST%2==0&&WST>=22,"");
#define NROT (NST/4)
constexpr int NST=56-2*WST, TST=WST+NST, SLC=TST%3;
#define FSD 8
#define FSW 128
#define FSX 128
#define SD0 0
#define SLP 1
constexpr unsigned WS_EPOCH=0, WS_DQ=4096, WS_WVF=WS_DQ+32*16384, WS_XF=WS_WVF+896*128, WS_XAF=WS_XF+896*128;
constexpr unsigned WS_WQ=1u<<20, WQ_G=896*1024, WS_WV=WS_WQ+8*WQ_G, WS_XR=WS_WV+28*16*16384, WS_X=WS_XR+8*16*32768, WS_END=WS_X+224*32768;
constexpr unsigned WS_STAMP=64u<<20;
constexpr unsigned LDS_B=98304, BS=16640, VS=16384, LDS_POLL=148480, LDS_BYTES=163840;
#define YSC " sc1"
#define YSC2 YSC
#define PSC " sc1"
#define WSC " sc1"
#define PRIO 3
#define AROT 0
#define AQO 0
#define TSC " sc1"
#define PWSC ""
#define PORD 1
#define AWT 0
#define PFSC ""
#define PHB 1
#define EB 0
#define TWQ 0
#define CPF 1
#define CPN 10
#define ADLY 48
#define AEXP 1
constexpr unsigned SPIN_CAP=1u<<18;

template<int I> struct ic{ static constexpr int v=I; };
template<int S,int E,class F> DI void static_for(F&& f){ if constexpr(S<E){ f(ic<S>{}); static_for<S+1,E>(f); } }
DI unsigned pk_bf16(float lo,float hi){ f32x2 v={lo,hi}; bf2_t r=__builtin_convertvector(v,bf2_t); return __builtin_bit_cast(unsigned,r); }
DI f32x2 unp(unsigned x){ return (f32x2){__builtin_bit_cast(float,x<<16),__builtin_bit_cast(float,x&0xffff0000u)}; }
DI unsigned dq2(int q0,int q1,f32x2 z,f32x2 s){
  f32x2 t=unp(pk_bf16((float)q0,(float)q1))-z;
  f32x2 w=unp(pk_bf16(t[0],t[1]))*s;
  return pk_bf16(w[0],w[1]);
}
#define DMA4(SC,O2,O3) \
  asm volatile("s_mov_b32 m0, %0\n s_nop 0\n" \
   "global_load_lds_dwordx4 %2, %1 offset:0" SC "\n" \
   "global_load_lds_dwordx4 %3, %1 offset:1024" SC "\n" \
   "global_load_lds_dwordx4 %4, %1 offset:" O2 SC "\n" \
   "global_load_lds_dwordx4 %5, %1 offset:" O3 SC "\n" \
   ::"s"(m0v),"s"(base),"v"(o0),"v"(o1),"v"(o2),"v"(o3):"memory")
DI void dma4(unsigned m0v,const char* base,unsigned o0,unsigned o1,unsigned o2,unsigned o3){ DMA4("","2048","3072"); }
DI void dma4s(unsigned m0v,const char* base,unsigned o0){ unsigned o1=o0,o2=o0,o3=o0; DMA4(" sc1","2048","3072"); }
DI void dma4p(unsigned m0v,const char* base,unsigned o0,unsigned o2){ unsigned o1=o0,o3=o2; DMA4(" sc1","2080","3104"); }
DI const char* uni(const char* p){ u64 a=(u64)p; return (const char*)(((u64)(unsigned)__builtin_amdgcn_readfirstlane((unsigned)(a>>32))<<32)|(unsigned)__builtin_amdgcn_readfirstlane((unsigned)a)); }
DI u32x4 mk_srd(const void* p,unsigned bytes){ u64 a=(u64)p; return (u32x4){(unsigned)a,(unsigned)(a>>32)&0xffffu,bytes,0x00020000u}; }
#define ST16(SC) asm volatile("buffer_store_dwordx4 %0, %1, %2, %3 offen" SC "\n s_nop 1"::"v"(w),"v"(voff),"s"(srd),"s"(soff):"memory")
DI void st16s(u32x4 w,u32x4 srd,unsigned voff,unsigned soff){ ST16(" sc1"); }
DI void st16p(u32x4 w,u32x4 srd,unsigned voff,unsigned soff){ ST16(PSC); }
DI void st16w(u32x4 w,u32x4 srd,unsigned voff,unsigned soff){ ST16(WSC); }
DI void st16y(u32x4 w,u32x4 srd,unsigned voff,unsigned soff){ ST16(YSC2); }
DI u32x4 ld16s_wait(const char* base,unsigned off){ u32x4 r; asm volatile("global_load_dwordx4 %0, %1, %2 sc1\n s_waitcnt vmcnt(0)":"=&v"(r):"v"(off),"s"(base):"memory"); return r; }
DI bool tag2(u32x4 v,unsigned tl,unsigned th){ return v[0]==tl&&v[1]==th&&v[2]==tl&&v[3]==th; }
DI void wait_flags(const char* base,int n16,unsigned tl,unsigned th,int lane){
  unsigned off=(lane<n16?lane:0)*16; base=uni(base);
  #pragma unroll 1
  for(unsigned it=0;it<SPIN_CAP;it++){
    u32x4 v=ld16s_wait(base,off);
    if(__builtin_amdgcn_ballot_w64(!tag2(v,tl,th))==0) break;
    __builtin_amdgcn_s_sleep(1);
  }
}
#define CLOB_32_127 "v32","v33","v34","v35","v36","v37","v38","v39","v40","v41","v42","v43","v44","v45","v46","v47","v48","v49","v50","v51","v52","v53","v54","v55","v56","v57","v58","v59","v60","v61","v62","v63","v64","v65","v66","v67","v68","v69","v70","v71","v72","v73","v74","v75","v76","v77","v78","v79","v80","v81","v82","v83","v84","v85","v86","v87","v88","v89","v90","v91","v92","v93","v94","v95","v96","v97","v98","v99","v100","v101","v102","v103","v104","v105","v106","v107","v108","v109","v110","v111","v112","v113","v114","v115","v116","v117","v118","v119","v120","v121","v122","v123","v124","v125","v126","v127"
#define ACC_OUT "=&{v[128:143]}"(acc[0]),"=&{v[144:159]}"(acc[1]),"=&{v[160:175]}"(acc[2]),"=&{v[176:191]}"(acc[3]),"=&{v[192:207]}"(acc[4]),"=&{v[208:223]}"(acc[5]),"=&{v[224:239]}"(acc[6]),"=&{v[240:255]}"(acc[7])
#define RING_S [slot]"+&s"(slot),[cnt]"+&s"(cnt),[dA]"=&s"(dA),[dB]"=&s"(dB)
#define RING_V [a0]"+&v"(a0),[a1]"+&v"(a1),[b0]"+&v"(b0),[b1]"+&v"(b1),[b2]"+&v"(b2),[b3]"+&v"(b3)
#define RING_K [kW]"s"(-65536),[kS]"s"(32768),[kWB]"s"(-2*(int)BS),[kSB]"s"((int)BS)
#define RING_KB [kW]"s"(-65536),[kS]"s"(32768),[kWB]"s"(-3*(int)VS),[kSB]"s"((int)VS)

#define TR(i)
#define TRDUMP()
#define STC 0
#define TRB(i)
#define TRE()
#define TRBDUMP()
#define STAMP(i)
#define STAMPL(i)
#define STAMPF(i)
#define STAMPT(i,t)

DI void dq_pos(int mt,unsigned v,unsigned& t,unsigned& d0,unsigned& dst,unsigned& hs){
  const unsigned nw=v>=16*WST?~0u:0u, vn=v-16*WST;
  const unsigned tn=(vn>>4)+(mt&1)*NROT;
  t=(((mt&1)*(WST/2)+(v>>5))&~nw)|((WST+(tn>=NST/2?tn-NST/2:tn))&nw);
  d0=(mt^((v>>4)&1&~nw))*128+8*(v&15);
  dst=(((v>>5)*65536+(v&31)*1024)&~nw)|((WST*32768+(vn>>4)*32768+(vn&15)*1024)&nw);
  hs=15+nw;
}
constexpr unsigned fl_off(unsigned k){ return k<16?k*FS0:16*FS0+(k-16)*FSD; }
DI unsigned dq_flag(unsigned v){ const unsigned vn=v-16*WST; return v<16*WST?((v>>3)&3)*16384+fl_off((v>>5)*8+(v&7)):((vn>>2)&3)*16384+fl_off(WST*4+(vn>>4)*4+(vn&3)); }
DI void dq_lda(const int* qw,const u16* sc,int mt,unsigned v,int lane,i32x8& qlo,u32x4& scv){
  unsigned t,d0,dst,hs; dq_pos(mt,v,t,d0,dst,hs);
  asm volatile("buffer_load_dwordx4 v[96:99], %2, %4, %6 offen nt\n buffer_load_dwordx4 v[100:103], %2, %4, %6 offen offset:256 nt\n"
    "buffer_load_dwordx2 v[112:113], %3, %5, %7 offen\n buffer_load_dwordx2 v[114:115], %3, %5, %7 offen offset:128\n s_nop 1"
    :"=&{v[96:103]}"(qlo),"=&{v[112:115]}"(scv):"v"((lane>>4)*14336+(lane&15)*16),"v"((lane&15)*8),"s"(mk_srd(qw,1024*3584*4)),"s"(mk_srd(sc,32*3584*2)),
     "s"(d0*14336+t*512),"s"((d0>>5)*7168+t*256):"memory");
}
DI void dq_ldb(const int* qw,const u16* zr,int mt,unsigned v,int lane,i32x8& qhi,u32x4& zrv){
  unsigned t,d0,dst,hs; dq_pos(mt,v,t,d0,dst,hs);
  asm volatile("buffer_load_dwordx2 v[116:117], %3, %5, %7 offen\n buffer_load_dwordx2 v[118:119], %3, %5, %7 offen offset:128\n"
    "buffer_load_dwordx4 v[104:107], %2, %4, %6 offen nt\n buffer_load_dwordx4 v[108:111], %2, %4, %6 offen offset:256 nt\n s_nop 1"
    :"=&{v[104:111]}"(qhi),"=&{v[116:119]}"(zrv):"v"((lane>>4)*14336+(lane&15)*16),"v"((lane&15)*8),"s"(mk_srd(qw,1024*3584*4)),"s"(mk_srd(zr,32*3584*2)),
     "s"((d0+4)*14336+t*512),"s"((d0>>5)*7168+t*256):"memory");
}
#define DQ_IO "+{v[64:79]}"(zs),"+{v[80:87]}"(tm),"+{v[88:91]}"(w0),"+{v[92:95]}"(w1),"+{v[96:103]}"(qlo),"+{v[104:111]}"(qhi),"+{v[112:115]}"(scv),"+{v[116:119]}"(zrv)
template<int O> DI void dq_put(u32x4 rs,int mt,unsigned v,int lane,u32x4 w){
  unsigned t,d0,dst,hs; dq_pos(mt,v,t,d0,dst,hs);
  const unsigned vo=(((lane>>3)&1)<<hs)+(lane>>4)*128+((((lane&7)^(lane>>5))<<4)^((v&1)<<6));
  st16w(w,rs,vo^(O*32),WS_WQ+mt*WQ_G+dst+O*512);
}
#define POLL(R) asm volatile("s_mov_b64 exec, %3\n global_load_dwordx2 %0, %1, %2 sc1\n s_mov_b64 exec, -1":"=&{" R "}"(r):"v"(off),"s"(base),"s"((u64)m):"memory")
template<int K> DI u64 poll(const char* base,unsigned off,unsigned m){ u64 r; if(K==0) POLL("v[240:241]"); else if(K==1) POLL("v[242:243]"); else if(K==2) POLL("v[244:245]"); else POLL("v[246:247]"); return r; }
template<int K> DI unsigned land(u64& r,u64 tag){ if(K==0) asm volatile("":"+{v[240:241]}"(r)); else if(K==1) asm volatile("":"+{v[242:243]}"(r)); else if(K==2) asm volatile("":"+{v[244:245]}"(r)); else asm volatile("":"+{v[246:247]}"(r));
  return (unsigned)__builtin_amdgcn_ballot_w64(r==tag); }
DI void spin(const char* base,unsigned off,unsigned m,u64 tag){
  #pragma unroll 1
  for(unsigned it=0;it<SPIN_CAP;it++){
    u64 r=poll<3>(base,off,m); WAITV(0);
    if((land<3>(r,tag)&m)==m) break;
    __builtin_amdgcn_s_sleep(SLP);
  }
}
template<int K> DI unsigned landh(u64& r,u64 tag){ (void)land<K>(r,tag); return (unsigned)__builtin_amdgcn_ballot_w64(r==tag||r==~tag)&1; }
DI void spinh(const char* base,unsigned off,u64 tag){
  #pragma unroll 1
  for(unsigned it=0;it<SPIN_CAP;it++){
    u64 r=poll<3>(base,off,1); WAITV(0);
    if(landh<3>(r,tag)) break;
    __builtin_amdgcn_s_sleep(SLP);
  }
}
typedef __amdgpu_buffer_rsrc_t rsrc_t;
DI rsrc_t mk_rs(const void* p,unsigned bytes){ return __builtin_amdgcn_make_buffer_rsrc((void*)p,0,bytes,0x00020000); }
template<int NT> struct DQ{ u32x4 q[4*NT]; u32x2 sz[4]; };
template<int NT> DI void dqn_issue(const int* qw,const u16* sc,const u16* zr,int mt,unsigned v,int lane,DQ<NT>& d){
  unsigned t,d0,dst,hs; dq_pos(mt,v,t,d0,dst,hs);
  const unsigned vq=(lane>>4)*14336+(lane&15)*16, vs=(lane&15)*8;
  const unsigned so=d0*14336+t*512, sg=(d0>>5)*7168+t*256;
  const rsrc_t rq=mk_rs(qw,1024*3584*4), rc=mk_rs(sc,32*3584*2), rz=mk_rs(zr,32*3584*2);
  d.sz[0]=__builtin_amdgcn_raw_buffer_load_b64(rc,vs,sg,0); d.sz[1]=__builtin_amdgcn_raw_buffer_load_b64(rc,vs+128,sg,0);
  d.sz[2]=__builtin_amdgcn_raw_buffer_load_b64(rz,vs,sg,0); d.sz[3]=__builtin_amdgcn_raw_buffer_load_b64(rz,vs+128,sg,0);
  #pragma unroll
  for(int k=0;k<2*NT;k++){ d.q[2*k]=__builtin_amdgcn_raw_buffer_load_b128(rq,vq,so+k*4*14336,2); d.q[2*k+1]=__builtin_amdgcn_raw_buffer_load_b128(rq,vq+256,so+k*4*14336,2); }
  asm volatile("":::"memory"); __builtin_amdgcn_sched_barrier(0);
}
template<int NT> DI void dqn_finish(u32x4 rw,int mt,unsigned v,int lane,DQ<NT>& d){
  unsigned t,d0,dst,hs; dq_pos(mt,v,t,d0,dst,hs);
  const unsigned vo=(((lane>>3)&1)<<hs)+(lane>>4)*128+((((lane&7)^(lane>>5))<<4)^((v&1)<<6));
  f32x2 sf[4],zf[4];
  #pragma unroll
  for(int e=0;e<4;e++){ sf[e]=unp(d.sz[e>>1][e&1]); zf[e]=unp(d.sz[2+(e>>1)][e&1]); }
  #pragma unroll
  for(int k=0;k<2*NT;k++){ const u32x4 a=d.q[2*k], b=d.q[2*k+1];
    u32x4 w={dq2(a[0],a[1],zf[0],sf[0]),dq2(a[2],a[3],zf[1],sf[1]),dq2(b[0],b[1],zf[2],sf[2]),dq2(b[2],b[3],zf[3],sf[3])};
    st16w(w,rw,vo^((k&3)*32),WS_WQ+mt*WQ_G+dst+k*512);
  }
  asm volatile("":::"memory"); __builtin_amdgcn_sched_barrier(0);
}
DI u64 ld_epoch(const char* ws){ u64 r; asm volatile("global_load_dwordx2 %0, %1, %2 sc1":"=&{v[120:121]}"(r):"v"(0),"s"(ws):"memory"); return r; }
DI u64 fin_epoch(u64 r){ asm volatile("":"+{v[120:121]}"(r)); return (u64)uni((const char*)(r+1)); }
DI void flag_st(const char* p,u64 tag){ asm volatile("s_mov_b64 exec, 1\n global_store_dwordx2 %0, %1, %2 sc1\n s_mov_b64 exec, -1"::"v"(0),"v"(tag),"s"(p):"memory"); }

DI int blk(int mt,int q,int h){ return PORD?((mt+4*h+(q&2))&7)^(q&1):(mt+2*q+h)&7; }
__global__ __launch_bounds__(512) void quip_fused(const char* __restrict__ x,const int* __restrict__ qw,const u16* __restrict__ sc,const u16* __restrict__ zr,
    const char* __restrict__ U,const char* __restrict__ V,char* __restrict__ y,char* ws){
  asm volatile(".Lquip_s:\n"
    "s_load_dwordx2 s[64:65], %[ka], 0x8\n s_load_dwordx2 s[68:69], %[ka], 0x10\n s_load_dwordx2 s[72:73], %[ka], 0x18\n s_load_dwordx2 s[76:77], %[ka], 0x38\n"
    "v_readfirstlane_b32 s80, %[tid]\n"
    "s_cmpk_ge_u32 %[bid], 224\n s_cbranch_scc1 .Lquip_k\n"
    "s_cmpk_lt_u32 s80, 256\n s_cbranch_scc1 .Lquip_k\n"
    "s_bfe_u32 s82, %[bid], 0x20001\n s_mulk_i32 s82, 28\n s_lshr_b32 s83, %[bid], 5\n s_lshl2_add_u32 s82, s83, s82\n"
    "s_bfe_u32 s83, s80, 0x20006\n s_add_u32 s82, s82, s83\n"
    "s_and_b32 s84, %[bid], 1\n s_bfe_u32 s85, %[bid], 0x20003\n s_lshl1_add_u32 s85, s85, s84\n"
    "s_bfe_u32 s86, s82, 0x10004\n s_xor_b32 s85, s85, s86\n s_and_b32 s86, s82, 15\n s_lshl4_add_u32 s85, s85, s86\n"
    "s_mulk_i32 s84, %[hw]\n s_lshr_b32 s86, s82, 5\n s_add_u32 s84, s84, s86\n"
    "s_mul_i32 s86, s85, 114688\n s_lshl_b32 s87, s84, 9\n s_add_u32 s86, s86, s87\n"
    "s_lshr_b32 s85, s85, 2\n s_mulk_i32 s85, 7168\n s_lshl_b32 s87, s84, 8\n s_add_u32 s85, s85, s87\n"
    "v_bfe_u32 v124, %[tid], 4, 2\n v_mul_u32_u24 v124, 14336, v124\n v_and_b32 v125, 15, %[tid]\n v_lshl_or_b32 v124, v125, 4, v124\n v_lshlrev_b32 v125, 3, v125\n v_mov_b32 v126, 0\n"
    "s_add_u32 s87, s86, 57344\n"
    "s_mov_b32 s66, 14680064\n s_mov_b32 s67, 0x20000\n s_mov_b32 s70, 229376\n s_mov_b32 s71, s67\n s_mov_b32 s74, s70\n s_mov_b32 s75, s67\n"
    "s_waitcnt lgkmcnt(0)\n"
    "s_and_b32 s65, s65, 0xffff\n"
    "buffer_load_dwordx4 v[96:99], v124, s[64:67], s86 offen nt\n buffer_load_dwordx4 v[100:103], v124, s[64:67], s86 offen offset:256 nt\n"
    "s_and_b32 s69, s69, 0xffff\n s_and_b32 s73, s73, 0xffff\n"
    "buffer_load_dwordx2 v[112:113], v125, s[68:71], s85 offen\n buffer_load_dwordx2 v[114:115], v125, s[68:71], s85 offen offset:128\n"
    "buffer_load_dwordx2 v[116:117], v125, s[72:75], s85 offen\n buffer_load_dwordx2 v[118:119], v125, s[72:75], s85 offen offset:128\n"
    "buffer_load_dwordx4 v[104:107], v124, s[64:67], s87 offen nt\n buffer_load_dwordx4 v[108:111], v124, s[64:67], s87 offen offset:256 nt\n"
    "global_load_dwordx2 v[120:121], v126, s[76:77] sc1\n"
    ".Lquip_k:\n s_waitcnt lgkmcnt(0)\n s_nop 1"
    ::[ka]"s"(__builtin_amdgcn_kernarg_segment_ptr()),[tid]"v"(threadIdx.x),[bid]"s"(blockIdx.x),[hw]"i"(WST/2)
    :"memory","scc","s64","s65","s66","s67","s68","s69","s70","s71","s72","s73","s74","s75","s76","s77","s80","s82","s83","s84","s85","s86","s87",
     "v96","v97","v98","v99","v100","v101","v102","v103","v104","v105","v106","v107","v108","v109","v110","v111","v112","v113","v114","v115","v116","v117","v118","v119","v120","v121","v124","v125","v126");
  __builtin_amdgcn_sched_barrier(0);
  const int lane=threadIdx.x&63; const int wid=__builtin_amdgcn_readfirstlane(threadIdx.x>>6);
  const int bid=blockIdx.x, xcd=bid&7, idx=bid>>3;
  const bool isA=idx>=28;
  const int mt=isA?xcd:(idx&3)*2+(xcd&1), nt=isA?(idx-28):(xcd>>1)*7+(idx>>2);
  if(!ENT&&!isA&&wid>=4){ i32x8 a,b; u32x4 c,d; dq_lda(qw,sc,mt,nt*4+wid-4,lane,a,c); dq_ldb(qw,zr,mt,nt*4+wid-4,lane,b,d); (void)ld_epoch(ws); }
  const u32x4 rs=mk_srd(ws,WS_END);
  const unsigned l16=lane*16;
  STAMPT(0,t00);
  if(AEXP?__builtin_expect_with_probability(isA,1,0.9):isA){
    __builtin_amdgcn_s_sleep(ADLY);
    const u64 tag=(u64)uni((const char*)__hip_atomic_load((GAS u64*)(ws+WS_EPOCH),__ATOMIC_RELAXED,__HIP_MEMORY_SCOPE_AGENT))+1;
    __builtin_amdgcn_sched_barrier(0);
    const int lw=wid-4;
    int slot=0;
    unsigned ao[4],bo[4];
    #pragma unroll
    for(int i=0;i<4;i++){ ao[i]=(64*lw+8*i+(lane>>3))*2048+(((lane&7)^((4*i+(lane>>4))&7))<<4)-i*1024;
      int g=((lane>>4)<<2)|(((i>>1)&1)<<1); bo[i]=(16*lw+4*i+(lane>>4))*2048+(((lane&15)^g)<<4)-i*1024; }
    const char* Ax=x+mt*256*2048;
    auto stage=[&](int t){
      const int tk=(t+AROT*nt)&15; const char* Ap=Ax+tk*128; const char* Bp=U+tk*131072+blk(mt,nt,t>>4)*256;
      dma4(LDS_B+slot*BS+lw*4096,Bp,bo[0],bo[1],bo[2],bo[3]);
      dma4(slot*32768+lw*8192,Ap,ao[0],ao[1],ao[2],ao[3]);
      dma4(slot*32768+lw*8192+4096,Ap+65536,ao[0],ao[1],ao[2],ao[3]);
      slot=slot==2?0:slot+1; };
    auto adq=[&](){
    if(AQ&&(wid&3)*8+(wid>>2)*4+nt<28){
      DQ<4> d; const unsigned v=112*AQ+((wid&3)*8+(wid>>2)*4+nt)*4;
      int ln=lane; asm volatile("":"+v"(ln));
      dqn_issue(qw,sc,zr,mt,v,ln,d); dqn_finish(rs,mt,v,ln,d);
      WAITV(0);
      asm volatile("s_mov_b64 exec, 15\n global_store_dwordx2 %0, %1, %2 sc1\n s_mov_b64 exec, -1"::"v"(ln*FSD),"v"(tag),"s"(uni(ws+WS_DQ+mt*65536+dq_flag(v))):"memory");
      STAMP(1);
    }
    };
    if(wid>=4){
      if(AQO) adq();
      stage(0); stage(1); stage(2);
      if(!AQO) adq();
      WAITV(24);
      BAR; TR(0);
      for(int s=0;s<29;s++){ WAITV(12); BAR; TR(s+1); stage(s+3); }
      WAITV(12); BAR; TR(30); WAITV(0); BAR; TR(31); BAR; TR(32); TRDUMP();
    } else {
      adq();
      const int wm=wid>>1, wn=wid&1, g4=lane>>4, r16=lane&15;
      unsigned a0,a1,b0,b1,b2,b3; int dA,dB;
      { int row=wm*64+r16; a0=row*128+((g4^((row>>1)&7))<<4); a1=a0^64;
        int q=r16>>2,p=lane&3,g=(q<<2)|((g4&1)<<1); unsigned bb=LDS_B+(8*g4+q)*256+8*(p&1); int ch=wn*8+(p>>1);
        b0=bb+((ch^g)<<4); b1=bb+(((ch+2)^g)<<4); b2=bb+(((ch+4)^g)<<4); b3=bb+(((ch+6)^g)<<4); }
      f32x16 acc[8];
      const char* df=ws+WS_XAF+mt*128+(nt*4+wid)*8;
      BAR;
      STAMP(5);
      for(int job=0;job<2;job++){
        int cnt=16;
        asm volatile(ASM_A : ACC_OUT, RING_V, RING_S : RING_K,[sd]"s"(job?14:-1),[xo]"v"(l16),[tag]"v"(tag),[df]"s"(df) : "memory","scc",CLOB_32_127);
        STAMP(6);
        if(AWT&&job) spin(uni(ws+WS_WVF+(((xcd>>1)*7+nt+(AWT-1))*4+wid)*8*FSW+mt*FSW),0,1,tag);
        const unsigned so=WS_XR+(mt*16+blk(mt,nt,job)*2+wn)*32768+wm*8192;
        #pragma unroll
        for(int m=0;m<8;m++)
        #pragma unroll
        for(int p=0;p<2;p++){ const f32x16& a=acc[m];
          u32x4 w={pk_bf16(a[8*p],a[8*p+1]),pk_bf16(a[8*p+2],a[8*p+3]),pk_bf16(a[8*p+4],a[8*p+5]),pk_bf16(a[8*p+6],a[8*p+7])};
          st16s(w,rs,l16,so+(m<4?m*2048:16384+(m-4)*2048)+p*1024); }
      }
      STAMP(7);
      WAITV(0); flag_st(df+1024,tag);
      asm volatile(".Lquip_e0:");
      STAMP(8);
    }
  } else if(wid>=4){
    const int lw=wid-4;
    i32x8 qlo,qhi,tm; u32x4 scv,zrv,w0,w1; u64 tag; f32x16 zs;
    asm volatile("":"={v[64:79]}"(zs),"={v[80:87]}"(tm),"={v[88:91]}"(w0),"={v[92:95]}"(w1));
    asm volatile("":"={v[96:103]}"(qlo),"={v[104:111]}"(qhi),"={v[112:115]}"(scv),"={v[116:119]}"(zrv),"={v[120:121]}"(tag));
    const unsigned v0=nt*4+lw;
    unsigned bo[4];
    #pragma unroll
    for(int i=0;i<4;i++){ int g=((lane>>4)<<2)|(((i>>1)&1)<<1); bo[i]=(64*(i&1)+8*lw+4*(i>>1)+(lane>>4))*7168+(((lane&15)^g)<<4)+3072-i*1024; }
    const char* Bp=V-3072+nt*256+(mt&1)*(WST*64*7168);
    const char* dqf=ws+WS_DQ+mt*65536;
    auto stageV=[&](int t){ if(t==WST) Bp=V-3072+nt*256+(2*WST+(mt&1)*2*NROT)*64*7168;
      if(t==TST-2*NROT&&NROT) Bp-=(mt&1)*NST*64*7168;
      dma4(LDS_B+(t&3)*VS+lw*4096,Bp,bo[0],bo[1],bo[2],bo[3]); Bp+=(t&1?96:32)*7168; };
    unsigned pfd=0;
    auto pfT=[&](int b,int m,int turn,int ph=-1){ int n=b-WST+(mt&1)*2*NROT; n=n>=NST?n-NST:n;
      const unsigned L=(idx>>2)*128*m+lw*32*m+(lane&(32*m-1));
      const unsigned so=(b<WST?(mt&1)*WST+b:2*WST+n)*(64*7168)+(xcd>>1)*1792;
      asm volatile("buffer_load_dword %0, %1, %2, %3 offen":"+{v122}"(pfd):"v"(L/14*7168+L%14*128),"s"(mk_srd(V,(idx&3)==turn&&(ph<0||((xcd>>PHB)&1)==ph)?3584*7168:0)),"s"(so):"memory"); };
    STAMP(9);
    WAITV(0);
    STAMP(10);
    if(EB) BAR;
    if(EV>0) stageV(0);
    if(EV>1) stageV(1);
    asm volatile(DQ_UNP DQ_SEQ0:DQ_IO); dq_put<0>(rs,mt,v0,lane,w0);
    if(ENT!=2){ asm volatile(DQ_SEQ1:DQ_IO); dq_put<1>(rs,mt,v0,lane,w1); }
    STAMP(1);
    tag=fin_epoch(tag);
    const unsigned tl=(unsigned)tag, th=(unsigned)(tag>>32);
    WAITV(0);
    if(ENT==2) BAR;
    flag_st(dqf+dq_flag(v0),tag);
    STAMP(2);
    if(!EB&&ENT!=2) BAR;
    u64 p0,p1,p2; unsigned got=0;
    const char* myf=dqf+lw*16384;
    const char* Ap=ws+WS_WQ+mt*WQ_G+lw*8192;
    auto stageW=[&](int sl){ dma4s(sl*32768+lw*8192,Ap,l16); dma4s(sl*32768+lw*8192+4096,Ap+4096,l16); Ap+=32768; };
    auto stageN=[&](int sl){ dma4s(sl*32768+lw*4096,Ap,l16); Ap+=16384; };
    const char* xfo=ws+WS_XF+(mt*28+nt)*4*FSX; const char* xa0=ws+WS_XAF+mt*128;
    const char* Xr=ws+WS_XR+mt*16*32768+lw*8192; const char* Wv=ws+WS_WV+nt*16*16384+lw*4096-32;
    auto kb=[&](int j){ return 2*blk(mt,(j+1)&3,(j+1)>>2&1); };
    auto stageA=[&](unsigned k,int sl){ const char* p=Xr+k*32768; dma4s(sl*32768+lw*8192,p,l16); dma4s(sl*32768+lw*8192+4096,p+4096,l16); };
    auto stageB=[&](unsigned k,int sl){ dma4p(LDS_B+sl*BS+lw*4160,Wv+k*16384,l16+32,l16); };
    u32x4 po[8];
    auto pub_rd=[&](){
      #pragma unroll
      for(int i=0;i<8;i++) po[i]=*(LAS u32x4*)(size_t)(LDS_B+(SLC+(i>>2))%3*BS+lw*4160+((i>>1)&1)*2080+(i&1)*1024+l16); };
    auto pub_st=[&](int h){
      #pragma unroll
      for(int i=4*h;i<4*h+4;i++) st16p(po[i],rs,l16,WS_WV+(nt*16+2*mt+(i>>2))*16384+lw*4096+(i&3)*1024); };
    auto publish=[&](){ pub_rd(); pub_st(0); pub_st(1); };
    const char* wf=ws+WS_WVF+(nt*4+lw)*8*FSW;
    const unsigned wo=kb(lane&7)/2*FSW;
    stageV(0);
    stageV(1);
    if(SD0) __builtin_amdgcn_s_sleep(SD0);
    spin(myf,lane*FS0,0xffff,tag); STAMP(3);
    stageW(0);
    stageW(1);
    stageW(2);
    WAITV(16);
    BAR; STAMP(4); TRB(0);
    stageV(2);
    stageV(3);
    WAITV(16);
    BAR; TRB(1);
    p2=poll<2>(myf+fl_off(16),lane*FSD,0xffu);
    stageW(0);
    stageV(4);
    dq_lda(qw,sc,mt,v0+224,lane,qlo,scv);
    dq_ldb(qw,zr,mt,v0+224,lane,qhi,zrv);
    pfT(6,2,0,-1);
    TRE();
    WAITV(26);
    BAR; TRB(2);
    WAITV(21);
    if((land<2>(p2,tag)&0xffu)!=0xffu) spin(myf+fl_off(16),lane*FSD,0xffu,tag);
    p0=poll<0>(myf+fl_off(24),lane*FSD,0xffffffffu);
    stageW(1);
    stageV(5);
    TRE();
    WAITV(26);
    BAR; TRB(3);
    stageW(2);
    stageV(6);
    WAITV(26);
    asm volatile("v_lshlrev_b32 v64, 16, v116\n" "v_and_b32 v65, 0xffff0000, v116\n" "v_lshlrev_b32 v72, 16, v112\n" "v_and_b32 v73, 0xffff0000, v112\n" "v_lshlrev_b32 v66, 16, v117\n" "v_and_b32 v67, 0xffff0000, v117\n" "v_lshlrev_b32 v74, 16, v113\n" "v_and_b32 v75, 0xffff0000, v113\n" "v_lshlrev_b32 v68, 16, v118\n" "v_and_b32 v69, 0xffff0000, v118\n" "v_lshlrev_b32 v76, 16, v114\n" "v_and_b32 v77, 0xffff0000, v114\n" "v_lshlrev_b32 v70, 16, v119\n" "v_and_b32 v71, 0xffff0000, v119\n" "v_lshlrev_b32 v78, 16, v115\n" "v_and_b32 v79, 0xffff0000, v115\n" "v_cvt_f32_i32 v80, v96\n" "v_cvt_f32_i32 v81, v97\n" "v_cvt_f32_i32 v82, v98\n" "v_cvt_f32_i32 v83, v99\n" "v_cvt_f32_i32 v84, v100\n" "v_cvt_f32_i32 v85, v101\n" "v_cvt_f32_i32 v86, v102\n" "v_cvt_f32_i32 v87, v103\n" "v_cvt_pk_bf16_f32 v80, 0, v80\n" "v_cvt_pk_bf16_f32 v81, 0, v81\n":DQ_IO);
    pfT(8,2,1,-1);
    TRE();
    WAITV(17);
    BAR; TRB(4);
    if((land<0>(p0,tag)&0xffffffffu)!=0xffffffffu) spin(myf+fl_off(24),lane*FSD,0xffffffffu,tag);
    stageW(0);
    stageV(7);
    asm volatile("v_cvt_pk_bf16_f32 v82, 0, v82\n" "v_cvt_pk_bf16_f32 v83, 0, v83\n" "v_cvt_pk_bf16_f32 v84, 0, v84\n" "v_cvt_pk_bf16_f32 v85, 0, v85\n" "v_cvt_pk_bf16_f32 v86, 0, v86\n" "v_cvt_pk_bf16_f32 v87, 0, v87\n" "v_sub_f32 v80, v80, v64\n" "v_sub_f32 v81, v81, v65\n" "v_sub_f32 v82, v82, v66\n" "v_sub_f32 v83, v83, v67\n" "v_sub_f32 v84, v84, v68\n" "v_sub_f32 v85, v85, v69\n" "v_sub_f32 v86, v86, v70\n" "v_sub_f32 v87, v87, v71\n" "v_cvt_pk_bf16_f32 v80, 0, v80\n" "v_cvt_pk_bf16_f32 v81, 0, v81\n" "v_cvt_pk_bf16_f32 v82, 0, v82\n" "v_cvt_pk_bf16_f32 v83, 0, v83\n" "v_cvt_pk_bf16_f32 v84, 0, v84\n" "v_cvt_pk_bf16_f32 v85, 0, v85\n" "v_cvt_pk_bf16_f32 v86, 0, v86\n" "v_cvt_pk_bf16_f32 v87, 0, v87\n" "v_mul_f32 v80, v80, v72\n" "v_mul_f32 v81, v81, v73\n" "v_mul_f32 v82, v82, v74\n" "v_mul_f32 v83, v83, v75\n":DQ_IO);
    TRE();
    WAITV(17);
    BAR; TRB(5);
    stageW(1);
    stageV(8);
    asm volatile("v_mul_f32 v84, v84, v76\n" "v_mul_f32 v85, v85, v77\n" "v_mul_f32 v86, v86, v78\n" "v_mul_f32 v87, v87, v79\n" "v_cvt_pk_bf16_f32 v88, v80, v81\n" "v_cvt_pk_bf16_f32 v89, v82, v83\n" "v_cvt_pk_bf16_f32 v90, v84, v85\n" "v_cvt_pk_bf16_f32 v91, v86, v87\n":DQ_IO);
    dq_put<0>(rs,mt,v0+224,lane,w0);
    asm volatile("v_cvt_f32_i32 v80, v104\n" "v_cvt_f32_i32 v81, v105\n" "v_cvt_f32_i32 v82, v106\n" "v_cvt_f32_i32 v83, v107\n" "v_cvt_f32_i32 v84, v108\n" "v_cvt_f32_i32 v85, v109\n" "v_cvt_f32_i32 v86, v110\n" "v_cvt_f32_i32 v87, v111\n" "v_cvt_pk_bf16_f32 v80, 0, v80\n" "v_cvt_pk_bf16_f32 v81, 0, v81\n" "v_cvt_pk_bf16_f32 v82, 0, v82\n" "v_cvt_pk_bf16_f32 v83, 0, v83\n" "v_cvt_pk_bf16_f32 v84, 0, v84\n" "v_cvt_pk_bf16_f32 v85, 0, v85\n" "v_cvt_pk_bf16_f32 v86, 0, v86\n" "v_cvt_pk_bf16_f32 v87, 0, v87\n" "v_sub_f32 v80, v80, v64\n" "v_sub_f32 v81, v81, v65\n":DQ_IO);
    pfT(10,2,2,-1);
    TRE();
    WAITV(18);
    BAR; TRB(6);
    stageW(2);
    stageV(9);
    asm volatile("v_sub_f32 v82, v82, v66\n" "v_sub_f32 v83, v83, v67\n" "v_sub_f32 v84, v84, v68\n" "v_sub_f32 v85, v85, v69\n" "v_sub_f32 v86, v86, v70\n" "v_sub_f32 v87, v87, v71\n" "v_cvt_pk_bf16_f32 v80, 0, v80\n" "v_cvt_pk_bf16_f32 v81, 0, v81\n" "v_cvt_pk_bf16_f32 v82, 0, v82\n" "v_cvt_pk_bf16_f32 v83, 0, v83\n" "v_cvt_pk_bf16_f32 v84, 0, v84\n" "v_cvt_pk_bf16_f32 v85, 0, v85\n" "v_cvt_pk_bf16_f32 v86, 0, v86\n" "v_cvt_pk_bf16_f32 v87, 0, v87\n" "v_mul_f32 v80, v80, v72\n" "v_mul_f32 v81, v81, v73\n" "v_mul_f32 v82, v82, v74\n" "v_mul_f32 v83, v83, v75\n" "v_mul_f32 v84, v84, v76\n" "v_mul_f32 v85, v85, v77\n" "v_mul_f32 v86, v86, v78\n" "v_mul_f32 v87, v87, v79\n" "v_cvt_pk_bf16_f32 v92, v80, v81\n" "v_cvt_pk_bf16_f32 v93, v82, v83\n" "v_cvt_pk_bf16_f32 v94, v84, v85\n" "v_cvt_pk_bf16_f32 v95, v86, v87\n":DQ_IO);
    dq_put<1>(rs,mt,v0+224,lane,w1);
    TRE();
    WAITV(19);
    BAR; TRB(7);
    stageW(0);
    stageV(10);
    dq_lda(qw,sc,mt,v0+336,lane,qlo,scv);
    dq_ldb(qw,zr,mt,v0+336,lane,qhi,zrv);
    pfT(12,2,3,-1);
    TRE();
    WAITV(26);
    BAR; TRB(8);
    WAITV(21);
    flag_st(dqf+dq_flag(v0+224),tag);
    stageW(1);
    stageV(11);
    TRE();
    WAITV(26);
    BAR; TRB(9);
    stageW(2);
    stageV(12);
    WAITV(26);
    asm volatile("v_lshlrev_b32 v64, 16, v116\n" "v_and_b32 v65, 0xffff0000, v116\n" "v_lshlrev_b32 v72, 16, v112\n" "v_and_b32 v73, 0xffff0000, v112\n" "v_lshlrev_b32 v66, 16, v117\n" "v_and_b32 v67, 0xffff0000, v117\n" "v_lshlrev_b32 v74, 16, v113\n" "v_and_b32 v75, 0xffff0000, v113\n" "v_lshlrev_b32 v68, 16, v118\n" "v_and_b32 v69, 0xffff0000, v118\n" "v_lshlrev_b32 v76, 16, v114\n" "v_and_b32 v77, 0xffff0000, v114\n" "v_lshlrev_b32 v70, 16, v119\n" "v_and_b32 v71, 0xffff0000, v119\n" "v_lshlrev_b32 v78, 16, v115\n" "v_and_b32 v79, 0xffff0000, v115\n" "v_cvt_f32_i32 v80, v96\n" "v_cvt_f32_i32 v81, v97\n" "v_cvt_f32_i32 v82, v98\n" "v_cvt_f32_i32 v83, v99\n" "v_cvt_f32_i32 v84, v100\n" "v_cvt_f32_i32 v85, v101\n" "v_cvt_f32_i32 v86, v102\n" "v_cvt_f32_i32 v87, v103\n" "v_cvt_pk_bf16_f32 v80, 0, v80\n" "v_cvt_pk_bf16_f32 v81, 0, v81\n":DQ_IO);
    pfT(14,2,0,-1);
    TRE();
    WAITV(17);
    BAR; TRB(10);
    p0=poll<0>(myf+fl_off(56),lane*FSD,0xffffffu);
    stageW(0);
    stageV(13);
    asm volatile("v_cvt_pk_bf16_f32 v82, 0, v82\n" "v_cvt_pk_bf16_f32 v83, 0, v83\n" "v_cvt_pk_bf16_f32 v84, 0, v84\n" "v_cvt_pk_bf16_f32 v85, 0, v85\n" "v_cvt_pk_bf16_f32 v86, 0, v86\n" "v_cvt_pk_bf16_f32 v87, 0, v87\n" "v_sub_f32 v80, v80, v64\n" "v_sub_f32 v81, v81, v65\n" "v_sub_f32 v82, v82, v66\n" "v_sub_f32 v83, v83, v67\n" "v_sub_f32 v84, v84, v68\n" "v_sub_f32 v85, v85, v69\n" "v_sub_f32 v86, v86, v70\n" "v_sub_f32 v87, v87, v71\n" "v_cvt_pk_bf16_f32 v80, 0, v80\n" "v_cvt_pk_bf16_f32 v81, 0, v81\n" "v_cvt_pk_bf16_f32 v82, 0, v82\n" "v_cvt_pk_bf16_f32 v83, 0, v83\n" "v_cvt_pk_bf16_f32 v84, 0, v84\n" "v_cvt_pk_bf16_f32 v85, 0, v85\n" "v_cvt_pk_bf16_f32 v86, 0, v86\n" "v_cvt_pk_bf16_f32 v87, 0, v87\n" "v_mul_f32 v80, v80, v72\n" "v_mul_f32 v81, v81, v73\n" "v_mul_f32 v82, v82, v74\n" "v_mul_f32 v83, v83, v75\n":DQ_IO);
    TRE();
    WAITV(18);
    BAR; TRB(11);
    stageW(1);
    stageV(14);
    asm volatile("v_mul_f32 v84, v84, v76\n" "v_mul_f32 v85, v85, v77\n" "v_mul_f32 v86, v86, v78\n" "v_mul_f32 v87, v87, v79\n" "v_cvt_pk_bf16_f32 v88, v80, v81\n" "v_cvt_pk_bf16_f32 v89, v82, v83\n" "v_cvt_pk_bf16_f32 v90, v84, v85\n" "v_cvt_pk_bf16_f32 v91, v86, v87\n":DQ_IO);
    dq_put<0>(rs,mt,v0+336,lane,w0);
    asm volatile("v_cvt_f32_i32 v80, v104\n" "v_cvt_f32_i32 v81, v105\n" "v_cvt_f32_i32 v82, v106\n" "v_cvt_f32_i32 v83, v107\n" "v_cvt_f32_i32 v84, v108\n" "v_cvt_f32_i32 v85, v109\n" "v_cvt_f32_i32 v86, v110\n" "v_cvt_f32_i32 v87, v111\n" "v_cvt_pk_bf16_f32 v80, 0, v80\n" "v_cvt_pk_bf16_f32 v81, 0, v81\n" "v_cvt_pk_bf16_f32 v82, 0, v82\n" "v_cvt_pk_bf16_f32 v83, 0, v83\n" "v_cvt_pk_bf16_f32 v84, 0, v84\n" "v_cvt_pk_bf16_f32 v85, 0, v85\n" "v_cvt_pk_bf16_f32 v86, 0, v86\n" "v_cvt_pk_bf16_f32 v87, 0, v87\n" "v_sub_f32 v80, v80, v64\n" "v_sub_f32 v81, v81, v65\n":DQ_IO);
    pfT(16,2,1,-1);
    TRE();
    WAITV(18);
    BAR; TRB(12);
    if((land<0>(p0,tag)&0xffffffu)!=0xffffffu) spin(myf+fl_off(56),lane*FSD,0xffffffu,tag);
    stageW(2);
    stageV(15);
    asm volatile("v_sub_f32 v82, v82, v66\n" "v_sub_f32 v83, v83, v67\n" "v_sub_f32 v84, v84, v68\n" "v_sub_f32 v85, v85, v69\n" "v_sub_f32 v86, v86, v70\n" "v_sub_f32 v87, v87, v71\n" "v_cvt_pk_bf16_f32 v80, 0, v80\n" "v_cvt_pk_bf16_f32 v81, 0, v81\n" "v_cvt_pk_bf16_f32 v82, 0, v82\n" "v_cvt_pk_bf16_f32 v83, 0, v83\n" "v_cvt_pk_bf16_f32 v84, 0, v84\n" "v_cvt_pk_bf16_f32 v85, 0, v85\n" "v_cvt_pk_bf16_f32 v86, 0, v86\n" "v_cvt_pk_bf16_f32 v87, 0, v87\n" "v_mul_f32 v80, v80, v72\n" "v_mul_f32 v81, v81, v73\n" "v_mul_f32 v82, v82, v74\n" "v_mul_f32 v83, v83, v75\n" "v_mul_f32 v84, v84, v76\n" "v_mul_f32 v85, v85, v77\n" "v_mul_f32 v86, v86, v78\n" "v_mul_f32 v87, v87, v79\n" "v_cvt_pk_bf16_f32 v92, v80, v81\n" "v_cvt_pk_bf16_f32 v93, v82, v83\n" "v_cvt_pk_bf16_f32 v94, v84, v85\n" "v_cvt_pk_bf16_f32 v95, v86, v87\n":DQ_IO);
    dq_put<1>(rs,mt,v0+336,lane,w1);
    TRE();
    WAITV(19);
    BAR; TRB(13);
    stageW(0);
    stageV(16);
    pfT(18,2,2,-1);
    TRE();
    WAITV(18);
    BAR; TRB(14);
    WAITV(13);
    flag_st(dqf+dq_flag(v0+336),tag);
    stageW(1);
    stageV(17);
    TRE();
    WAITV(18);
    BAR; TRB(15);
    stageW(2);
    stageV(18);
    pfT(20,2,3,-1);
    TRE();
    WAITV(17);
    BAR; TRB(16);
    p0=poll<0>(myf+fl_off(80),lane*FSD,0xffffffffu);
    stageW(0);
    stageV(19);
    TRE();
    WAITV(18);
    BAR; TRB(17);
    stageW(1);
    stageV(20);
    pfT(22,2,0,-1);
    TRE();
    WAITV(17);
    BAR; TRB(18);
    if((land<0>(p0,tag)&0xffffffffu)!=0xffffffffu) spin(myf+fl_off(80),lane*FSD,0xffffffffu,tag);
    stageW(2);
    stageV(21);
    TRE();
    WAITV(17);
    BAR; TRB(19);
    stageW(0);
    stageV(22);
    pfT(24,2,1,-1);
    TRE();
    WAITV(17);
    BAR; TRB(20);
    stageW(1);
    stageV(23);
    TRE();
    WAITV(17);
    BAR; TRB(21);
    stageW(2);
    stageV(24);
    pfT(26,2,2,-1);
    TRE();
    WAITV(17);
    BAR; TRB(22);
    Ap-=lw*4096;
    stageN(0);
    stageV(25);
    TRE();
    WAITV(13);
    BAR; TRB(23);
    stageN(1);
    stageV(26);
    TRE();
    WAITV(12);
    BAR; TRB(24);
    stageN(2);
    stageV(27);
    TRE();
    WAITV(12);
    BAR; TRB(25);
    stageN(0);
    stageV(28);
    TRE();
    WAITV(12);
    BAR; TRB(26);
    stageN(1);
    stageV(29);
    TRE();
    WAITV(12);
    BAR; TRB(27);
    p0=poll<0>(xfo,lane*FSX,0xfu);
    stageN(2);
    stageV(30);
    TRE();
    WAITV(13);
    BAR; TRB(28);
    p2=poll<2>(xa0,lane*8,0xffffu);
    stageN(0);
    stageV(31);
    TRE();
    WAITV(13);
    if((land<0>(p0,tag)&0xfu)!=0xfu) spin(xfo,lane*FSX,0xfu,tag);
    BAR; TRB(29);
    stageN(1);
    TRE();
    WAITV(8);
    BAR; TRB(30);
    if((land<2>(p2,tag)&0xffffu)!=0xffffu) spin(xa0,lane*8,0xffffu,tag);
    stageA(kb(-1)+0,2);
    TRE();
    WAITV(8);
    BAR; TRB(31);
    stageA(kb(-1)+1,0);
    TRE();
    BAR; TRB(32);
    TRE();
    STAMPL(5);
    WAITV(8);
    BAR; TRB(33); STAMPL(11);
    publish();
    STAMPL(12);
    WAITV(8);
    BAR; TRB(34);
    WAITV(0);
    p1=poll<1>(wf,wo,0x1u);
    flag_st(wf+mt*FSW,tag);
    STAMPL(6);
    stageA(kb(0)+0,1);
    WAITV(9);
    got|=land<1>(p1,tag)&1;
    STAMPL(14); if(!got) spin(wf,wo,1,tag);
    STAMPL(7);
    stageB(kb(0)+0,1);
    p0=poll<0>(wf,wo,0x7fu);
    stageB(kb(0)+1,2);
    stageA(kb(0)+1,2);
    WAITV(13);
    STAMPL(15); BAR; TRB(35); STAMPL(8);
    WAITV(12);
    got|=land<0>(p0,tag)&0x7f;
    if(!(got>>1&1)) spin(wf,wo,2,tag);
    stageB(kb(1)+0,0);
    p0=poll<0>(wf,wo,0x7fu);
    stageA(kb(1)+0,0);
    TRE();
    WAITV(13);
    BAR; TRB(36);
    stageB(kb(1)+1,1);
    stageA(kb(1)+1,1);
    TRE();
    WAITV(12);
    BAR; TRB(37);
    got|=land<0>(p0,tag)&0x7f;
    if(!(got>>2&1)) spin(wf,wo,4,tag);
    stageB(kb(2)+0,2);
    p2=poll<2>(xa0+1024,lane*8,0xffffu);
    p0=poll<0>(wf,wo,0x7fu);
    stageA(kb(2)+0,2);
    TRE();
    WAITV(14);
    BAR; TRB(38);
    stageB(kb(2)+1,0);
    stageA(kb(2)+1,0);
    TRE();
    WAITV(12);
    BAR; TRB(39);
    if((land<2>(p2,tag)&0xffffu)!=0xffffu) spin(xa0+1024,lane*8,0xffffu,tag);
    got|=land<0>(p0,tag)&0x7f;
    if(!(got>>3&1)) spin(wf,wo,8,tag);
    stageB(kb(3)+0,1);
    p0=poll<0>(wf,wo,0x7fu);
    stageA(kb(3)+0,1);
    TRE();
    WAITV(13);
    BAR; TRB(40);
    stageB(kb(3)+1,2);
    stageA(kb(3)+1,2);
    TRE();
    WAITV(12);
    BAR; TRB(41);
    got|=land<0>(p0,tag)&0x7f;
    if(!(got>>4&1)) spin(wf,wo,16,tag);
    stageB(kb(4)+0,0);
    p0=poll<0>(wf,wo,0x7fu);
    stageA(kb(4)+0,0);
    TRE();
    WAITV(13);
    BAR; TRB(42);
    stageB(kb(4)+1,1);
    stageA(kb(4)+1,1);
    TRE();
    WAITV(12);
    BAR; TRB(43);
    got|=land<0>(p0,tag)&0x7f;
    if(!(got>>5&1)) spin(wf,wo,32,tag);
    stageB(kb(5)+0,2);
    p0=poll<0>(wf,wo,0x7fu);
    stageA(kb(5)+0,2);
    TRE();
    WAITV(13);
    BAR; TRB(44);
    stageB(kb(5)+1,0);
    stageA(kb(5)+1,0);
    TRE();
    WAITV(12);
    BAR; TRB(45);
    got|=land<0>(p0,tag)&0x7f;
    if(!(got>>6&1)) spin(wf,wo,64,tag);
    stageB(kb(6)+0,1);
    stageA(kb(6)+0,1);
    TRE();
    WAITV(12);
    BAR; TRB(46);
    stageB(kb(6)+1,2);
    stageA(kb(6)+1,2);
    TRE();
    WAITV(12);
    BAR; TRB(47);
    WAITV(0);
    BAR; TRB(48); TRB(49); STAMPF(5); STAMPF(6); STAMPF(7); STAMPF(8); STAMPF(11); STAMPF(12); STAMPF(13); STAMPF(14); STAMPF(15);

    asm volatile(".Lquip_e1:"::"{v122}"(pfd));
    TRBDUMP();
    if(bid==0&&lw==0){ wait_flags(ws+WS_XAF+1024,64,tl,th,lane);
      if(lane==0) __hip_atomic_store((GAS u64*)(ws+WS_EPOCH),((u64)th<<32)|tl,__ATOMIC_RELAXED,__HIP_MEMORY_SCOPE_AGENT); }
  } else {
    const int wm=wid>>1, wn=wid&1, g4=lane>>4, r16=lane&15;
    const char* df=ws+WS_DQ+mt*65536+dq_flag(112+nt*4+wid);
    u64 tag;
    { DQ<1> d1; const unsigned v=112+nt*4+wid;
      unsigned cp=0;
      if(CPF){
        u64 pc; unsigned o0,e0,e1,e2;
        asm volatile("s_getpc_b64 %0\n.Lquip_pc:\n s_mov_b32 %1, .Lquip_pc-.Lquip_s\n s_mov_b32 %2, .Lquip_e0-.Lquip_s\n s_mov_b32 %3, .Lquip_e1-.Lquip_s\n s_mov_b32 %4, .Lquip_e2-.Lquip_s":"=s"(pc),"=s"(o0),"=s"(e0),"=s"(e1),"=s"(e2));
        pc-=o0;
        const unsigned o=min((unsigned)((idx*4+wid)*CPN+min(lane,CPN-1))*32,max(e0,max(e1,e2)));
        asm volatile("global_load_dword %0, %1, %2":"=v"(cp):"v"(o),"s"(pc):"memory"); }
      if(ENT==2){
        i32x8 qlo,qhi,tm; u32x4 scv,zrv,w0,w1; f32x16 zs;
        asm volatile("":"={v[64:79]}"(zs),"={v[80:87]}"(tm),"={v[88:91]}"(w0),"={v[92:95]}"(w1));
        asm volatile("":"={v[96:103]}"(qlo),"={v[104:111]}"(qhi),"={v[112:115]}"(scv),"={v[116:119]}"(zrv));
        WAITV(CPF?2:1);
        asm volatile(DQ_UNP DQ_SEQ0:DQ_IO); dq_put<1>(rs,mt,nt*4+wid,lane,w0);
        WAITV(0); }
      unsigned tq=0;
      if(TWQ) asm volatile("buffer_load_dword %0, %1, %2, 0 offen sc1":"=v"(tq):"v"(WS_WQ+mt*WQ_G+min(wid*4+min(lane,3),13)*65536),"s"(rs):"memory");
      BAR;
      dqn_issue(qw,sc,zr,mt,v,lane,d1);
      tag=__hip_atomic_load((GAS u64*)(ws+WS_EPOCH),__ATOMIC_RELAXED,__HIP_MEMORY_SCOPE_AGENT)+1;
      dqn_finish(rs,mt,v,lane,d1);
      asm volatile(""::"v"(cp),"v"(tq));
      STAMP(1);
    }
    unsigned a0,a1,b0,b1,b2,b3; int slot=0,slb=0,dA,dB;
    { int row=wm*64+r16; a0=row*128+((g4^((row>>1)&7))<<4); a1=a0^64;
      int q=r16>>2,p=lane&3,g=(q<<2)|((g4&1)<<1); unsigned bb=LDS_B+(8*g4+q)*256+8*(p&1); int ch=wn*8+(p>>1);
      b0=bb+((ch^g)<<4); b1=bb+(((ch+2)^g)<<4); b2=bb+(((ch+4)^g)<<4); b3=bb+(((ch+6)^g)<<4); }
    f32x16 acc[8];
    const char* xf=ws+WS_XF+(((mt^1)*28+nt)*4+wid)*FSX;
    const char* xs=ws+WS_X+((mt^1)*28+nt)*32768+wid*8192+4096;
    const char* xl=ws+WS_X+(mt*28+nt)*32768+wid*8192+4096;
    const unsigned one=(r16>>2)==g4?((r16&1)?0x3f800000u:0x3f80u):0u, d0=(r16&2)?0u:one, d1=(r16&2)?one:0u;
    __builtin_amdgcn_s_setprio(PRIO);
    BAR;
    STAMP(3);
    { int cnt=WST; unsigned pd;
      asm volatile(ASM_B : ACC_OUT, RING_V, RING_S, [slb]"+&s"(slb), [pd]"=&v"(pd) : RING_KB,[po]"v"(lane*128),[xo]"v"(l16),[tag]"v"(tag),[df]"s"(df),[xf]"s"(xf),[xs]"s"(xs),[xl]"s"(xl),[d0]"v"(d0),[d1]"v"(d1),[pw]"s"(ws+WS_WV+(nt*16+2*mt)*16384+wid*8192+4096),
        [nA0]"v"(a0),[nA1]"v"(a1),[nH0]"v"(a0+65536),[nH1]"v"(a1+65536),[nB0]"v"(b0),[nB1]"v"(b1),[nB2]"v"(b2),[nB3]"v"(b3) : "memory","scc",CLOB_32_127); }
    STAMPL(4);
    unsigned lb=l16; asm volatile("":"+v"(lb));
    { const unsigned base=LDS_B+(wm==0?SLC:(SLC+1)%3)*BS+((lb>>4)&7)*2080+(lb&0x300)+wn*128+((lb>>3)&16);
      #pragma unroll
      for(int p=0;p<2;p++)
      #pragma unroll
      for(int n=0;n<4;n++){ const f32x16& a=acc[2*p]; const f32x16& b=acc[2*p+1];
        *(LAS u32x4*)(size_t)(base+p*1024+n*32)=(u32x4){pk_bf16(a[4*n],a[4*n+1]),pk_bf16(a[4*n+2],a[4*n+3]),pk_bf16(b[4*n],b[4*n+1]),pk_bf16(b[4*n+2],b[4*n+3])}; }
    }
    asm volatile("s_waitcnt lgkmcnt(0)":::"memory");
    STAMPL(9);
    BAR;
    STAMPL(5);
    a0=SLC*32768+wid*8192+lb; b0=LDS_B+SLC*BS+lb; slot=SLC;
    const u32x4 ry=mk_srd(y,2048*7168);
    const unsigned offY=(mt*256+wid*64+(lb>>8)*4)*7168+nt*256+(lb&0xf0);
    { int cnt=CTAIL?13:16;
      asm volatile(ASM_C : ACC_OUT, [a0]"+&v"(a0),[b0]"+&v"(b0), RING_S : RING_K,[tA]"v"(wid*8192+lb),[tH]"v"(65536+wid*8192+lb),[tB]"v"(LDS_B+lb),[oy]"v"(offY),[ry]"s"(ry) : "memory","scc",CLOB_32_127); }
    STAMP(6);
    #pragma unroll
    for(int i=CTAIL*8;i<16;i++){
      u32x4 w={pk_bf16(acc[0][i],acc[1][i]),pk_bf16(acc[2][i],acc[3][i]),pk_bf16(acc[4][i],acc[5][i]),pk_bf16(acc[6][i],acc[7][i])};
      st16y(w,ry,offY,((i>>2)*16+(i&3))*7168);
    }
    asm volatile(".Lquip_e2:":::"memory");
    STAMP(7);
  }
}

extern "C" void kernel_launch(void* const* d_in,const int* in_sizes,int n_in,void* d_out,int out_size,void* d_ws,size_t ws_size,hipStream_t stream){
  (void)hipFuncSetAttribute((const void*)quip_fused,hipFuncAttributeMaxDynamicSharedMemorySize,LDS_BYTES);
  quip_fused<<<256,512,LDS_BYTES,stream>>>((const char*)d_in[0],(const int*)d_in[1],(const u16*)d_in[2],(const u16*)d_in[3],(const char*)d_in[4],(const char*)d_in[5],(char*)d_out,(char*)d_ws);
}
```
